```python
import torch

def _softmax(x, axis=-1):
    return torch.softmax(x, dim=axis)
def _gelu(x):
    return torch.nn.functional.gelu(x, approximate="tanh")
def _silu(x):
    return torch.nn.functional.silu(x)
def _sigmoid(x):
    return torch.sigmoid(x)
def _layernorm(x, axis=-1, eps=1e-5):
    m = x.mean(dim=axis, keepdim=True)
    v = x.var(dim=axis, keepdim=True, unbiased=False)
    return (x - m) / torch.sqrt(v + eps)
def _scatter_add(tgt, idx, src, axis=0):
    return tgt.clone().index_add_(axis, idx, src)
def _segment_sum(src, seg, n):
    out = torch.zeros((n,) + src.shape[1:], dtype=src.dtype)
    return out.index_add_(0, seg, src)
def _pad(x, pw):
    flat = tuple(v for pair in reversed(pw) for v in pair)
    return torch.nn.functional.pad(x, flat)

kernel_name = 'recurrent_scan__gqa_block_cached__dense_activation_relu__decode'

def setup_inputs():

    _w = (lambda a: (lambda t: t.to(torch.bfloat16) if t.is_floating_point() else t)(torch.as_tensor(a)))
    def _A_setup():
        B, Sq, D = 8, 1, 448
        Hq, Hkv, Sk, Dh = 16, 8, 4096, 256
        assert Hq % Hkv == 0, (Hq, Hkv)
        return {
            "x": _w((lambda *s: torch.randn(*s, dtype=torch.bfloat16) * 0.05)(B, Sq, D)),
            "k_cache": _w((lambda *s: torch.randn(*s, dtype=torch.bfloat16) * 0.8)(B, Hkv, Sk, Dh)),
            "v_cache": _w((lambda *s: torch.randn(*s, dtype=torch.bfloat16) * 0.8)(B, Hkv, Sk, Dh)),
            "wq": _w((lambda *s: torch.randn(*s, dtype=torch.bfloat16) * 0.8)(D, Hq * Dh) * (D ** -0.5)),
            "wo": _w((lambda *s: torch.randn(*s, dtype=torch.bfloat16) * 0.8)(Hq * Dh, D) * ((Hq * Dh) ** -0.5)),
        }
    _A_in = _A_setup()

    def _B_setup():
        B, S, D, Dff = 8, 1, 448, 1792
        return {
            "x": _w((lambda *s: torch.randn(*s, dtype=torch.bfloat16) * 0.05)(B, S, D)),
            "w1": _w((lambda *s: torch.randn(*s, dtype=torch.bfloat16) * 0.2)(D, Dff)),
            "w2": _w((lambda *s: torch.randn(*s, dtype=torch.bfloat16) * 0.2)(Dff, D)),
        }
    _B_in = _B_setup()

    return {
        "A_x": _A_in["x"],
        "A_k_cache": _A_in["k_cache"],
        "A_v_cache": _A_in["v_cache"],
        "A_wq": _A_in["wq"],
        "A_wo": _A_in["wo"],
        "B_x": _B_in["x"],
        "B_w1": _B_in["w1"],
        "B_w2": _B_in["w2"],
    }

def reference(A_x, A_k_cache, A_v_cache, A_wq, A_wo, B_x, B_w1, B_w2):
    def _A_ref(x, k_cache, v_cache, wq, wo):
        B, Sq, D = x.shape
        Hq, Dh = 16, 256
        Hkv = k_cache.shape[1]
        rep = Hq // Hkv
        q = (lambda x, *d: x.permute(*d))((x @ wq).reshape(B, Sq, Hq, Dh), 0, 2, 1, 3)
        k = (lambda x, n, axis: torch.repeat_interleave(x, n, dim=axis))(k_cache, rep, axis=1)
        v = (lambda x, n, axis: torch.repeat_interleave(x, n, dim=axis))(v_cache, rep, axis=1)
        scores = torch.einsum("bhqd,bhkd->bhqk", q, k) * (Dh ** -0.5)
        p = _softmax(scores, axis=-1)
        o = torch.einsum("bhqk,bhkd->bhqd", p, v)
        return (lambda x, *d: x.permute(*d))(o, 0, 2, 1, 3).reshape(B, Sq, Hq * Dh) @ wo

    def _B_ref(x, w1, w2):
        h = torch.einsum("bsd,df->bsf", x, w1)
        zero = torch.zeros_like(h)
        h = torch.maximum(h, zero)
        return torch.einsum("bsf,fd->bsd", h, w2)

    x = A_x
    xn = _layernorm(x, axis=-1)
    s = _A_ref(xn, A_k_cache, A_v_cache, A_wq, A_wo)
    g = _silu(_B_ref(xn, B_w1, B_w2))
    return x + s * g
```

```cpp
#include <hip/hip_runtime.h>
#include <hip/hip_bf16.h>
#include <stdint.h>

typedef unsigned u32;
typedef unsigned u32x4 __attribute__((ext_vector_type(4)));
typedef __attribute__((ext_vector_type(2))) __bf16 bf16x2;

constexpr int B = 8, D = 448, HQ = 16, HKV = 8, SK = 4096, DH = 256, DFF = 1792;
constexpr int NSPLIT = 16;
constexpr int KPS = SK / NSPLIT;

__device__ __forceinline__ float bf2f(unsigned short b) { return __uint_as_float((u32)b << 16); }
__device__ __forceinline__ float rbf(float f) {
  __bf16 b = (__bf16)f; return (float)b;
}
__device__ __forceinline__ float dot2(u32 k, u32 q, float acc) {
  return __builtin_amdgcn_fdot2_f32_bf16(__builtin_bit_cast(bf16x2, k), __builtin_bit_cast(bf16x2, q), acc, false);
}

__device__ void layernorm_to_lds(const unsigned short* x, float* xn, int tid, int nthr, float* red) {
  int wave = tid >> 6, lane = tid & 63, nw = nthr >> 6;
  for (int b = wave; b < B; b += nw) {
    float v[7]; float s = 0.f;
    for (int i = 0; i < 7; ++i) { v[i] = bf2f(x[b * D + lane + 64 * i]); s += v[i]; }
    for (int o = 32; o; o >>= 1) s += __shfl_xor(s, o);
    float mean = s / D;
    float ss = 0.f;
    for (int i = 0; i < 7; ++i) { float d = v[i] - mean; ss += d * d; }
    for (int o = 32; o; o >>= 1) ss += __shfl_xor(ss, o);
    float var = rbf(ss / D);
    float mb = rbf(mean);
    float sd = rbf(sqrtf(rbf(var + 1e-5f)));
    for (int i = 0; i < 7; ++i) xn[b * D + lane + 64 * i] = rbf(rbf(v[i] - mb) / sd);
  }
  __syncthreads();
}

__global__ void __launch_bounds__(256) k_proj(const unsigned short* x, const unsigned short* wq, const unsigned short* w1,
                                              unsigned short* q, float* h) {
  __shared__ float xn[B * D];
  __shared__ float red[8];
  layernorm_to_lds(x, xn, threadIdx.x, 256, red);
  int col = blockIdx.x * 64 + (threadIdx.x & 63);
  int part = threadIdx.x >> 6;
  const unsigned short* w; int ld; int c;
  if (col < HQ * DH) { w = wq; ld = HQ * DH; c = col; } else { w = w1; ld = DFF; c = col - HQ * DH; }
  float acc[B];
  for (int b = 0; b < B; ++b) acc[b] = 0.f;
  for (int k = part * 112; k < part * 112 + 112; ++k) {
    float wv = bf2f(w[(size_t)k * ld + c]);
    for (int b = 0; b < B; ++b) acc[b] = fmaf(xn[b * D + k], wv, acc[b]);
  }
  __shared__ float part_acc[4][B][64];
  for (int b = 0; b < B; ++b) part_acc[part][b][threadIdx.x & 63] = acc[b];
  __syncthreads();
  if (part == 0) {
    for (int b = 0; b < B; ++b) {
      float s = part_acc[0][b][threadIdx.x] + part_acc[1][b][threadIdx.x] + part_acc[2][b][threadIdx.x] + part_acc[3][b][threadIdx.x];
      if (col < HQ * DH) { __bf16 r = (__bf16)s; q[b * HQ * DH + col] = __builtin_bit_cast(unsigned short, r); }
      else h[b * DFF + c] = fmaxf(rbf(s), 0.f);
    }
  }
}

__global__ void __launch_bounds__(256) k_attn(const unsigned short* q, const unsigned short* kc, const unsigned short* vc,
                                              float* pml, float* po) {
  __shared__ float sc[2][KPS];
  __shared__ float red[16];
  __shared__ float accs[4][2][2][DH];
  int s = blockIdx.x, kvh = blockIdx.y, b = blockIdx.z;
  int tid = threadIdx.x, lane = tid & 63, wave = tid >> 6, r = lane >> 5, c = lane & 31;
  const u32x4* kp = (const u32x4*)(kc + ((size_t)(b * HKV + kvh) * SK + s * KPS) * DH);
  const u32x4* vp = (const u32x4*)(vc + ((size_t)(b * HKV + kvh) * SK + s * KPS) * DH);
  u32x4 q0 = *(const u32x4*)(q + (size_t)b * HQ * DH + (kvh * 2) * DH + 8 * c);
  u32x4 q1 = *(const u32x4*)(q + (size_t)b * HQ * DH + (kvh * 2 + 1) * DH + 8 * c);
  for (int i = wave * 2 + r; i < KPS; i += 8) {
    u32x4 kv = __builtin_nontemporal_load(kp + i * 32 + c);
    float d0 = 0.f, d1 = 0.f;
    for (int j = 0; j < 4; ++j) { d0 = dot2(kv[j], q0[j], d0); d1 = dot2(kv[j], q1[j], d1); }
    for (int o = 16; o; o >>= 1) { d0 += __shfl_xor(d0, o); d1 += __shfl_xor(d1, o); }
    if (c == 0) { sc[0][i] = rbf(d0) * 0.0625f; sc[1][i] = rbf(d1) * 0.0625f; }
  }
  __syncthreads();
  float m0 = sc[0][tid], m1 = sc[1][tid];
  float a0 = m0, a1 = m1;
  for (int o = 32; o; o >>= 1) { m0 = fmaxf(m0, __shfl_xor(m0, o)); m1 = fmaxf(m1, __shfl_xor(m1, o)); }
  if (lane == 0) { red[wave] = m0; red[4 + wave] = m1; }
  __syncthreads();
  m0 = fmaxf(fmaxf(red[0], red[1]), fmaxf(red[2], red[3]));
  m1 = fmaxf(fmaxf(red[4], red[5]), fmaxf(red[6], red[7]));
  float p0 = __expf(a0 - m0), p1 = __expf(a1 - m1);
  sc[0][tid] = p0; sc[1][tid] = p1;
  float l0 = p0, l1 = p1;
  for (int o = 32; o; o >>= 1) { l0 += __shfl_xor(l0, o); l1 += __shfl_xor(l1, o); }
  if (lane == 0) { red[8 + wave] = l0; red[12 + wave] = l1; }
  __syncthreads();
  l0 = red[8] + red[9] + red[10] + red[11];
  l1 = red[12] + red[13] + red[14] + red[15];
  float acc0[8], acc1[8];
  for (int j = 0; j < 8; ++j) { acc0[j] = 0.f; acc1[j] = 0.f; }
  for (int i = wave * 2 + r; i < KPS; i += 8) {
    u32x4 vv = __builtin_nontemporal_load(vp + i * 32 + c);
    float w0 = sc[0][i], w1 = sc[1][i];
    for (int j = 0; j < 4; ++j) {
      float lo = __uint_as_float(vv[j] << 16), hi = __uint_as_float(vv[j] & 0xffff0000u);
      acc0[2 * j] = fmaf(w0, lo, acc0[2 * j]); acc0[2 * j + 1] = fmaf(w0, hi, acc0[2 * j + 1]);
      acc1[2 * j] = fmaf(w1, lo, acc1[2 * j]); acc1[2 * j + 1] = fmaf(w1, hi, acc1[2 * j + 1]);
    }
  }
  for (int j = 0; j < 8; ++j) { accs[wave][r][0][8 * c + j] = acc0[j]; accs[wave][r][1][8 * c + j] = acc1[j]; }
  __syncthreads();
  for (int hh = 0; hh < 2; ++hh) {
    float o = 0.f;
    for (int w = 0; w < 4; ++w) o += accs[w][0][hh][tid] + accs[w][1][hh][tid];
    size_t row = (size_t)(b * HQ + kvh * 2 + hh);
    po[(row * NSPLIT + s) * DH + tid] = o;
    if (tid == 0) { pml[(row * NSPLIT + s) * 2] = hh ? m1 : m0; pml[(row * NSPLIT + s) * 2 + 1] = hh ? l1 : l0; }
  }
}

__global__ void __launch_bounds__(256) k_combine(const float* pml, const float* po, float* o) {
  size_t row = (size_t)blockIdx.y * HQ + blockIdx.x;
  float M = -INFINITY;
  for (int s = 0; s < NSPLIT; ++s) M = fmaxf(M, pml[(row * NSPLIT + s) * 2]);
  float L = 0.f, acc = 0.f;
  for (int s = 0; s < NSPLIT; ++s) {
    float w = __expf(pml[(row * NSPLIT + s) * 2] - M);
    L += w * pml[(row * NSPLIT + s) * 2 + 1];
    acc += w * po[(row * NSPLIT + s) * DH + threadIdx.x];
  }
  o[row * DH + threadIdx.x] = rbf(acc / L);
}

__global__ void __launch_bounds__(256) k_out(const unsigned short* x, const float* o, const float* h, const unsigned short* wo,
                                             const unsigned short* w2, unsigned short* out) {
  int b = blockIdx.y, col = blockIdx.x * 64 + (threadIdx.x & 63), part = threadIdx.x >> 6;
  float s = 0.f, g = 0.f;
  for (int k = part * 1024; k < part * 1024 + 1024; ++k) s = fmaf(o[b * 4096 + k], bf2f(wo[(size_t)k * D + col]), s);
  for (int k = part * 448; k < part * 448 + 448; ++k) g = fmaf(h[b * DFF + k], bf2f(w2[(size_t)k * D + col]), g);
  __shared__ float ps[4][64], pg[4][64];
  ps[part][threadIdx.x & 63] = s; pg[part][threadIdx.x & 63] = g;
  __syncthreads();
  if (part == 0) {
    int t = threadIdx.x;
    s = rbf(ps[0][t] + ps[1][t] + ps[2][t] + ps[3][t]);
    g = rbf(pg[0][t] + pg[1][t] + pg[2][t] + pg[3][t]);
    g = rbf(g / (1.f + __expf(-g)));
    float r = bf2f(x[b * D + col]) + rbf(s * g);
    __bf16 rb = (__bf16)r;
    out[b * D + col] = __builtin_bit_cast(unsigned short, rb);
  }
}

extern "C" void kernel_launch(void* const* d_in, const int* in_sizes, int n_in, void* d_out, int out_size, void* d_ws,
                              size_t ws_size, hipStream_t stream) {
  const unsigned short* x = (const unsigned short*)d_in[0];
  const unsigned short* kc = (const unsigned short*)d_in[1];
  const unsigned short* vc = (const unsigned short*)d_in[2];
  const unsigned short* wq = (const unsigned short*)d_in[3];
  const unsigned short* wo = (const unsigned short*)d_in[4];
  const unsigned short* w1 = (const unsigned short*)d_in[6];
  const unsigned short* w2 = (const unsigned short*)d_in[7];
  char* ws = (char*)d_ws;
  unsigned short* q = (unsigned short*)ws;
  float* h = (float*)(ws + 65536);
  float* o = (float*)(ws + 131072);
  float* pml = (float*)(ws + 262144);
  float* po = (float*)(ws + 524288);
  k_proj<<<(HQ * DH + DFF) / 64, 256, 0, stream>>>(x, wq, w1, q, h);
  k_attn<<<dim3(NSPLIT, HKV, B), 256, 0, stream>>>(q, kc, vc, pml, po);
  k_combine<<<dim3(HQ, B), 256, 0, stream>>>(pml, po, o);
  k_out<<<dim3(7, B), 256, 0, stream>>>(x, o, h, wo, w2, (unsigned short*)d_out);
}
```

```cpp
#include <hip/hip_runtime.h>
#include <hip/hip_bf16.h>
#include <stdint.h>

typedef unsigned u32;
typedef unsigned long long u64;
typedef unsigned short u16;
typedef unsigned u32x4 __attribute__((ext_vector_type(4)));
typedef unsigned u32x2 __attribute__((ext_vector_type(2)));
typedef float f32x4 __attribute__((ext_vector_type(4)));
typedef float f32x2 __attribute__((ext_vector_type(2)));
typedef short s16x4 __attribute__((ext_vector_type(4)));
typedef short s16x8 __attribute__((ext_vector_type(8)));
typedef __attribute__((ext_vector_type(2))) __bf16 bf16x2;
typedef __attribute__((ext_vector_type(8))) __bf16 bf16x8;
#define GAS __attribute__((address_space(1)))
#define LAS __attribute__((address_space(3)))
#define RLX_AGENT __ATOMIC_RELAXED, __HIP_MEMORY_SCOPE_AGENT

#define WIN 44
#define WAUX 2
#define H0_DELAY 100
#define HINT_G 20
#define HINT_C 0
#define SKIP 0
#define STAMPS 0

constexpr int B = 8, D = 448, HQ = 16, HKV = 8, SK = 4096, DH = 256, DFF = 1792;
constexpr int NC = 8, NTHR = (NC + 2) * 64;
constexpr int NSLOT = 32, NGRP = 22;
constexpr float CEXP = 0.0625f * 1.4426950408889634f;
constexpr unsigned SPIN_CAP = 1u << 18;

constexpr u32 WS_EPOCH = 0;
constexpr u32 WS_H0 = 65536;
constexpr u32 WS_ML = WS_H0 + 64 * 256 * 8;
constexpr u32 WS_HINT = WS_ML + 64 * 16 * 8;
constexpr u32 WS_H1 = WS_HINT + 64 * 256 * 8;
constexpr u32 WS_HF = WS_H1 + 32 * 8 * 3 * 128 * 8;
constexpr u32 WS_H2 = WS_HF + 4 * 28 * 56 * 32 * 8;
constexpr u32 WS_ST = WS_H2 + 4 * 28 * 32 * 32 * 8;

constexpr int L_RING = 0;
constexpr int L_Q = NSLOT * 4096;
constexpr int L_SC = L_Q + 1024;
constexpr int L_MX = L_SC + 8 * 1408;
constexpr int L_CTL = L_MX + 128;
constexpr int XN_STRIDE = 912, HA_STRIDE = 80, OA_STRIDE = 272;
constexpr int S_WQ = 0;
constexpr int S_W1 = S_WQ + 57344;
constexpr int S_W2 = S_W1 + 28672;
constexpr int S_XN = S_W2 + 28672;
constexpr int S_HA = S_XN + 8 * XN_STRIDE;
constexpr int S_OA = S_HA + 8 * HA_STRIDE;
constexpr int S_TAG = S_OA + 8 * OA_STRIDE;
constexpr int L_BYTES = L_CTL + 64 + 128;
static_assert(L_BYTES <= 160 * 1024 && S_TAG + 16 <= L_BYTES, "LDS");

__device__ __forceinline__ float bf2f(u32 b) { return __uint_as_float(b << 16); }
__device__ __forceinline__ u32 f2bf(float f) { __bf16 b = (__bf16)f; return (u32)__builtin_bit_cast(u16, b); }
__device__ __forceinline__ float rbf(float f) { return bf2f(f2bf(f)); }
__device__ __forceinline__ u32 pk2(float lo, float hi) {
  bf16x2 v = {(__bf16)lo, (__bf16)hi};
  return __builtin_bit_cast(u32, v);
}
__device__ __forceinline__ float dot2(u32 a, u32 b, float acc) {
  return __builtin_amdgcn_fdot2_f32_bf16(__builtin_bit_cast(bf16x2, a), __builtin_bit_cast(bf16x2, b), acc, false);
}
template <int CTRL> __device__ __forceinline__ float dpp(float x) {
  return __builtin_bit_cast(float, __builtin_amdgcn_mov_dpp(__builtin_bit_cast(int, x), CTRL, 0xf, 0xf, true));
}
template <int CTRL, int BANK> __device__ __forceinline__ float dppm(float old, float x) {
  return __builtin_bit_cast(float, __builtin_amdgcn_update_dpp(__builtin_bit_cast(int, old), __builtin_bit_cast(int, x), CTRL, 0xf, BANK, false));
}
constexpr int XOR1 = 0xB1, XOR2 = 0x4E, ROR4 = 0x124, ROR8 = 0x128, SHL4 = 0x104, SHR4 = 0x114, BCAST = 0x150;
__device__ __forceinline__ f32x2 swap16(float a, float b) {
  auto s = __builtin_amdgcn_permlane16_swap(__float_as_uint(a), __float_as_uint(b), false, false);
  return f32x2{__uint_as_float(s[0]), __uint_as_float(s[1])};
}
__device__ __forceinline__ f32x2 swap32(float a, float b) {
  auto s = __builtin_amdgcn_permlane32_swap(__float_as_uint(a), __float_as_uint(b), false, false);
  return f32x2{__uint_as_float(s[0]), __uint_as_float(s[1])};
}
__device__ __forceinline__ float xrows_sum(float x) {
  f32x2 s = swap16(x, x); x = s[0] + s[1];
  f32x2 t = swap32(x, x); return t[0] + t[1];
}
__device__ __forceinline__ float wave_sum(float x) {
  x += dpp<XOR1>(x); x += dpp<XOR2>(x); x += dpp<ROR4>(x); x += dpp<ROR8>(x);
  return xrows_sum(x);
}
typedef __amdgpu_buffer_rsrc_t rsrc_t;
__device__ __forceinline__ rsrc_t mk_rsrc(const void* p, u32 bytes) { return __builtin_amdgcn_make_buffer_rsrc((void*)p, 0, (int)bytes, 0x00020000); }
__device__ __forceinline__ u32x4 ldw(rsrc_t r, u32 voff, u32 soff) { return __builtin_amdgcn_raw_buffer_load_b128(r, voff, soff, WAUX); }
#define LGKM0() asm volatile("s_waitcnt lgkmcnt(0)" ::: "memory")
__device__ __forceinline__ void wg_barrier() {
  LGKM0();
  __builtin_amdgcn_s_barrier();
  asm volatile("" ::: "memory");
}
__device__ __forceinline__ void st_gran(GAS u64* g, u32 tag, u32 v) { __hip_atomic_store(g, ((u64)tag << 32) | (u64)v, RLX_AGENT); }
__device__ __forceinline__ u64 ld_gran(const GAS u64* g) { return __hip_atomic_load(g, RLX_AGENT); }
#define PSLEEP 1
__device__ __forceinline__ void lds_wait_ge(volatile LAS u32* p, u32 need) {
  for (unsigned spins = 0; (u32)__builtin_amdgcn_readfirstlane(*p) < need && spins < (SPIN_CAP << 4); ++spins) __builtin_amdgcn_s_sleep(PSLEEP);
  asm volatile("" ::: "memory");
}
#define PIN8(a) asm volatile("" : "+v"(a[0]), "+v"(a[1]), "+v"(a[2]), "+v"(a[3]), "+v"(a[4]), "+v"(a[5]), "+v"(a[6]), "+v"(a[7]))
#define PIN4(a) asm volatile("" : "+v"(a[0]), "+v"(a[1]), "+v"(a[2]), "+v"(a[3]) : : "memory")

#define STAMP(i) do { } while (0)

__device__ __forceinline__ float k_unit(const u32x4 (&k)[4], const u32x4& q0, const u32x4& q1) {
  float e[4];
#pragma unroll
  for (int i = 0; i < 4; ++i) {
    float a = 0.f, b = 0.f;
#pragma unroll
    for (int j = 0; j < 4; ++j) { a = dot2(k[i][j], q0[j], a); b = dot2(k[i][j], q1[j], b); }
    const f32x2 s = swap16(a, b);
    e[i] = s[0] + s[1];
  }
  const float f0 = dppm<ROR8, 0x3>(e[2], e[0]) + dppm<ROR8, 0xc>(e[0], e[2]);
  const float f1 = dppm<ROR8, 0x3>(e[3], e[1]) + dppm<ROR8, 0xc>(e[1], e[3]);
  float g = dppm<SHL4, 0x5>(f1, f0) + dppm<SHR4, 0xa>(f0, f1);
  g += dpp<XOR1>(g); g += dpp<XOR2>(g);
  return rbf(g);
}
__device__ __forceinline__ void v_piece(f32x2 (&acc)[8], float p0, float p1, u32x4 v) {
#pragma unroll
  for (int j = 0; j < 4; ++j) {
    const f32x2 vv = {__uint_as_float(v[j] << 16), __uint_as_float(v[j] & 0xffff0000u)};
    acc[j] = __builtin_elementwise_fma(vv, f32x2{p0, p0}, acc[j]);
    acc[4 + j] = __builtin_elementwise_fma(vv, f32x2{p1, p1}, acc[4 + j]);
  }
}

#define DMA_W(R, VO, SO, M0) asm volatile("s_mov_b32 m0, %0\n\ts_nop 0\n\tbuffer_load_dwordx4 %1, %2, %3 offen nt lds" :: "s"(M0), "v"(VO), "s"(R), "s"(SO) : "m0", "memory")
#define VM0() asm volatile("s_waitcnt vmcnt(0)" ::: "memory")
__device__ __forceinline__ s16x8 tr_frag(const LAS unsigned char* p, int four_rows) {
  const s16x4 b0 = __builtin_amdgcn_ds_read_tr16_b64_v4i16((LAS s16x4*)p);
  const s16x4 b1 = __builtin_amdgcn_ds_read_tr16_b64_v4i16((LAS s16x4*)(p + four_rows));
  return s16x8{b0[0], b0[1], b0[2], b0[3], b1[0], b1[1], b1[2], b1[3]};
}
__device__ __forceinline__ f32x4 mfma16(s16x8 a, s16x8 b, f32x4 c) {
  return __builtin_amdgcn_mfma_f32_16x16x32_bf16(__builtin_bit_cast(bf16x8, a), __builtin_bit_cast(bf16x8, b), c, 0, 0, 0);
}
template <int KS> __device__ __forceinline__ f32x4 gemm8x16(const LAS unsigned char* A, int astride, const LAS unsigned char* Bt, int bstride, int lane) {
  f32x4 acc = {0.f, 0.f, 0.f, 0.f};
  const int g = lane >> 4;
  const LAS unsigned char* ap = A + (lane & 7) * astride + 16 * g;
  const LAS unsigned char* bp = Bt + (8 * g + ((lane & 15) >> 2)) * bstride + 8 * (lane & 3);
#pragma unroll
  for (int ks = 0; ks < KS; ++ks) acc = mfma16(*(const LAS s16x8*)(ap + 64 * ks), tr_frag(bp + ks * 32 * bstride, 4 * bstride), acc);
  return acc;
}
__device__ __forceinline__ u32 pair_of(f32x4 acc, int lane, int& batch) {
  const int g = lane >> 4;
  const float lo = (g >> 1) ? acc[2] : acc[0], hi = (g >> 1) ? acc[3] : acc[1];
  const float send = (lane & 1) ? lo : hi, keep = (lane & 1) ? hi : lo;
  const float got = dpp<XOR1>(send);
  batch = 4 * (g & 1) + (g >> 1) * 2 + (lane & 1);
  return (lane & 1) ? pk2(got, keep) : pk2(keep, got);
}
__device__ __forceinline__ void st_gran2(rsrc_t wsr, u32 voff, u32 soff, u32 tag, float a, float b) {
  __builtin_amdgcn_raw_buffer_store_b128(u32x4{__float_as_uint(a), tag, __float_as_uint(b), tag}, wsr, voff + soff, 0, 16);
}
__device__ __forceinline__ void pub_tile(rsrc_t wsr, u32 soff, u32 pstride, f32x4 acc, u32 tag, int lane) {
  const int g = lane >> 4;
  st_gran2(wsr, (u32)(2 * (g & 1) + (g >> 1)) * pstride + (lane & 15) * 16, soff, tag, (g >> 1) ? acc[2] : acc[0], (g >> 1) ? acc[3] : acc[1]);
}
__device__ __forceinline__ void nap(int units) {
  for (int i = 0; i < units; ++i) __builtin_amdgcn_s_sleep(127);
}
template <int N, int SLEEP> __device__ __forceinline__ void sweep1(rsrc_t wsr, u32 voff, u32 soff, u32 stride, u32 tag, u32 (&v)[N]) {
  unsigned have = 0;
#pragma unroll
  for (int i = 0; i < N; ++i) v[i] = 0u;
  for (unsigned spins = 0;; ++spins) {
    u32x2 t[N];
    asm volatile("" ::: "memory");
#pragma unroll
    for (int i = 0; i < N; ++i) t[i] = __builtin_amdgcn_raw_buffer_load_b64(wsr, voff, ((have >> i) & 1u) ? 0x40000000u : soff + i * stride, 16);
#pragma unroll
    for (int i = 0; i < N; ++i) {
      v[i] = ((have >> i) & 1u) ? v[i] : t[i][0];
      have |= __all(t[i][1] == tag) ? 1u << i : 0u;
    }
    if (have == (1u << N) - 1u || spins > SPIN_CAP) break;
    __builtin_amdgcn_s_sleep(SLEEP);
  }
  asm volatile("" ::: "memory");
}
template <int N, int SLEEP> __device__ __forceinline__ void sweep2(rsrc_t wsr, u32 voff, u32 soff, u32 stride, u32 tag, f32x2 (&v)[N]) {
  unsigned have = 0;
#pragma unroll
  for (int i = 0; i < N; ++i) v[i] = f32x2{0.f, 0.f};
  for (unsigned spins = 0;; ++spins) {
    u32x4 t[N];
    asm volatile("" ::: "memory");
#pragma unroll
    for (int i = 0; i < N; ++i) t[i] = __builtin_amdgcn_raw_buffer_load_b128(wsr, voff, ((have >> i) & 1u) ? 0x40000000u : soff + i * stride, 16);
#pragma unroll
    for (int i = 0; i < N; ++i) {
      v[i] = ((have >> i) & 1u) ? v[i] : f32x2{__uint_as_float(t[i][0]), __uint_as_float(t[i][2])};
      have |= __all(t[i][1] == tag && t[i][3] == tag) ? 1u << i : 0u;
    }
    if (have == (1u << N) - 1u || spins > SPIN_CAP) break;
    __builtin_amdgcn_s_sleep(SLEEP);
  }
  asm volatile("" ::: "memory");
}

__global__ void __launch_bounds__(NTHR, 3)
fused_decode(const u16* __restrict__ x_, const u16* __restrict__ kc_, const u16* __restrict__ vc_, const u16* __restrict__ wq_,
             const u16* __restrict__ wo_, const u16* __restrict__ w1_, const u16* __restrict__ w2_, u16* __restrict__ out_,
             unsigned char* ws_) {
  extern __shared__ __attribute__((aligned(16))) unsigned char smem[];
  LAS unsigned char* L = (LAS unsigned char*)smem;
  GAS unsigned char* ws = (GAS unsigned char*)ws_;
  const int tid = threadIdx.x, lane = tid & 63, wave = __builtin_amdgcn_readfirstlane(tid >> 6);
  const int bid = blockIdx.x, kvh = bid & 7, jj = bid >> 3;
  const u32 ldsb = (u32)(uintptr_t)L;
  const rsrc_t wsr = mk_rsrc(ws_, WS_ST);

  if (jj >= 24) {
    const int t = jj - 24, c = wave - 1;
    const bool mid = wave >= 1 && wave <= 8;
    if (wave == 0) STAMP(0);
    u32 tag;
    if (mid) {
      u32x4 xw;
      asm volatile("global_load_dwordx4 %0, %1, off" : "=v"(xw) : "v"(x_ + c * D + 8 * (lane < 56 ? lane : 55)) : "memory");
      {
        const rsrc_t r = mk_rsrc(wq_, (SKIP & 8) ? 0u : (u32)D * HQ * DH * 2);
        const u32 vo = (lane >> 3) * (HQ * DH * 2) + (lane & 7) * 16;
#pragma unroll
        for (int it = 0; it < 7; ++it) DMA_W(r, vo, (56 * c + 8 * it) * (HQ * DH * 2) + kvh * 1024 + t * 128, ldsb + S_WQ + (7 * c + it) * 1024);
      }
      asm volatile("s_waitcnt vmcnt(7)" : "+v"(xw) :: "memory");
      float xv[8];
#pragma unroll
      for (int j = 0; j < 4; ++j) { xv[2 * j] = __uint_as_float(xw[j] << 16); xv[2 * j + 1] = __uint_as_float(xw[j] & 0xffff0000u); }
      float s = ((xv[0] + xv[1]) + (xv[2] + xv[3])) + ((xv[4] + xv[5]) + (xv[6] + xv[7]));
      const float mean = wave_sum(lane < 56 ? s : 0.f) * (1.f / D);
      float ss = 0.f;
#pragma unroll
      for (int j = 0; j < 8; ++j) { const float d = xv[j] - mean; ss = fmaf(d, d, ss); }
      const float var = rbf(wave_sum(lane < 56 ? ss : 0.f) * (1.f / D));
      const float mb = rbf(mean), sd = rbf(sqrtf(rbf(var + 1e-5f)));
      u32x4 o;
#pragma unroll
      for (int j = 0; j < 4; ++j) o[j] = pk2(rbf(xv[2 * j] - mb) / sd, rbf(xv[2 * j + 1] - mb) / sd);
      if (lane < 56) *(LAS u32x4*)(L + S_XN + c * XN_STRIDE + 16 * lane) = o;
      VM0();
    } else {
      if (wave == 0) {
        const u32 e = __hip_atomic_load((const GAS u32*)(ws + WS_EPOCH), RLX_AGENT);
        if (lane == 0) *(LAS u32*)(L + S_TAG) = e + 1u;
      }
    }
    wg_barrier();
    tag = __builtin_amdgcn_readfirstlane(*(const LAS u32*)(L + S_TAG));
    if (wave >= 1 && wave <= 4) {
      const f32x4 acc = gemm8x16<14>(L + S_XN, XN_STRIDE, L + S_WQ + 32 * c, 128, lane);
      int bb; const u32 v = pair_of(acc, lane, bb);
      st_gran((GAS u64*)(ws + WS_H0) + (size_t)(bb * 8 + kvh) * 256 + 32 * t + 8 * c + ((lane & 15) >> 1), tag, v);
      if (wave == 1) STAMP(9);
    } else if (t < 7) {
      const int w = wave == 0 ? 0 : wave - 4;
      const rsrc_t r1 = mk_rsrc(w1_, (u32)D * DFF * 2), r2 = mk_rsrc(w2_, (u32)D * DFF * 2);
      const u32 vo = (lane >> 2) * (DFF * 2) + (lane & 3) * 16;
      for (int it = w; it < ((SKIP & 1) ? 0 : 28); it += 6) DMA_W(r1, vo, it * 16 * (DFF * 2) + kvh * 448 + t * 64, ldsb + S_W1 + it * 1024);
      for (int it = w; it < ((SKIP & 1) ? 0 : 28); it += 6) DMA_W(r2, (u32)lane * 16, (kvh * 224 + t * 32) * (D * 2) + it * 1024, ldsb + S_W2 + it * 1024);
      VM0();
    }
    wg_barrier();
    if (mid) {
      const rsrc_t r = mk_rsrc(wo_, (u32)D * HQ * DH * 2);
#pragma unroll
      for (int it = 0; it < ((SKIP & 4) ? 0 : 7); ++it) {
        const int i = lane + 64 * (7 * c + it), row = i / 28;
        DMA_W(r, (u32)(row * (D * 2) + (i - row * 28) * 16), (4 * kvh + (t >> 1)) * 128 * (D * 2) + (t & 1) * 448, ldsb + S_WQ + (7 * c + it) * 1024);
      }
    } else if (t < 7) {
      const int half = wave != 0;
      f32x4 acc = gemm8x16<14>(L + S_XN, XN_STRIDE, L + S_W1 + 32 * half, 64, lane);
#pragma unroll
      for (int i = 0; i < 4; ++i) acc[i] = fmaxf(acc[i], 0.f);
      int bb; const u32 v = pair_of(acc, lane, bb);
      *(LAS u32*)(L + S_HA + bb * HA_STRIDE + half * 32 + 4 * ((lane & 15) >> 1)) = v;
    }
    wg_barrier();
    if (t < 7 && wave >= 1 && wave <= 7 && !(SKIP & 2)) {
#pragma unroll
      for (int i = 0; i < 4; ++i) {
        const f32x4 acc = gemm8x16<1>(L + S_HA, HA_STRIDE, L + S_W2 + 32 * (4 * c + i), D * 2, lane);
        pub_tile(wsr, WS_HF + ((4 * c + i) * 56 + kvh * 7 + t) * 256, 28 * 56 * 256, acc, tag, lane);
      }
    }
    VM0();
    wg_barrier();
    if (mid) {
      s16x8 wb[2][4];
      {
        const LAS unsigned char* bp = L + S_WQ + (8 * (lane >> 4) + ((lane & 15) >> 2)) * 448 + 64 * (c < 7 ? c : 6) + 8 * (lane & 3);
#pragma unroll
        for (int i = 0; i < 2; ++i)
#pragma unroll
          for (int ks = 0; ks < 4; ++ks) wb[i][ks] = tr_frag(bp + 32 * i + ks * 32 * 448, 4 * 448);
        asm volatile("" : "+v"(wb[0][0]), "+v"(wb[0][1]), "+v"(wb[0][2]), "+v"(wb[0][3]), "+v"(wb[1][0]), "+v"(wb[1][1]), "+v"(wb[1][2]), "+v"(wb[1][3]));
      }
      float w0, w1, w2;
      {
        nap(5);
        u32 v[1];
        sweep1<1, 100>(wsr, (u32)((lane & 15) < 12 ? (lane & 15) : 0) * 8, WS_ML + (c * 8 + kvh) * 128, 0, tag, v);
        const int h = t >> 2;
        float mv[3], lv[3];
#pragma unroll
        for (int s = 0; s < 3; ++s) {
          mv[s] = __uint_as_float(__builtin_amdgcn_readlane(v[0], 4 * s + h));
          lv[s] = __uint_as_float(__builtin_amdgcn_readlane(v[0], 4 * s + 2 + h));
        }
        const float M = fmaxf(fmaxf(mv[0], mv[1]), mv[2]);
        w0 = __builtin_amdgcn_exp2f((mv[0] - M) * CEXP); w1 = __builtin_amdgcn_exp2f((mv[1] - M) * CEXP); w2 = __builtin_amdgcn_exp2f((mv[2] - M) * CEXP);
        const float Ls = fmaf(w0, lv[0], fmaf(w1, lv[1], w2 * lv[2]));
        w0 /= Ls; w1 /= Ls; w2 /= Ls;
      }
      if (c == 0) STAMP(10);
      {
        u32 hv[1];
        sweep1<1, 16>(wsr, (u32)((lane & 3) < 3 ? (lane & 3) : 0) * 8, WS_HINT + (kvh * 8 + t) * 2048 + (kvh * 24 + c * 3) * 8, 0, tag, hv);
        f32x2 v[3];
        sweep2<3, 1>(wsr, lane * 16, WS_H1 + ((4 * kvh + (t >> 1)) * 8 + c) * 3072, 1024, tag, v);
        *(LAS u32*)(L + S_OA + c * OA_STRIDE + 4 * lane) =
            pk2(fmaf(w0, v[0][0], fmaf(w1, v[1][0], w2 * v[2][0])), fmaf(w0, v[0][1], fmaf(w1, v[1][1], w2 * v[2][1])));
      }
      if (c == 7) STAMP(6);
      wg_barrier();
      if (c < 7) {
        const LAS unsigned char* ap = L + S_OA + (lane & 7) * OA_STRIDE + 16 * (lane >> 4);
        f32x4 s0 = {0.f, 0.f, 0.f, 0.f}, s1 = {0.f, 0.f, 0.f, 0.f};
#pragma unroll
        for (int ks = 0; ks < 4; ++ks) { const s16x8 a = *(const LAS s16x8*)(ap + 64 * ks); s0 = mfma16(a, wb[0][ks], s0); s1 = mfma16(a, wb[1][ks], s1); }
        const u32 so = WS_H2 + ((14 * (t & 1) + 2 * c) * 32 + 4 * kvh + (t >> 1)) * 256;
        pub_tile(wsr, so, 28 * 32 * 256, s0, tag, lane);
        pub_tile(wsr, so + 32 * 256, 28 * 32 * 256, s1, tag, lane);
        if (c == 0) STAMP(21);
        if (STAMPS) { VM0(); if (c == 0) STAMP(22); }
      }
      return;
    }
    if (t == 7) return;
    const int f = 2 * (7 * kvh + t) + (wave != 0), bp = f / 28, ct = f - bp * 28;
    f32x2 gate, xres;
    {
      const GAS u16* xp = (const GAS u16*)x_ + 2 * bp * D + 16 * ct + (lane & 15);
      xres = f32x2{bf2f(xp[0]), bf2f(xp[D])};
      nap(1);
      f32x2 v[14];
      if (SKIP & 2) { for (int i = 0; i < 14; ++i) v[i] = xres; } else
      sweep2<14, 100>(wsr, lane * 16, WS_HF + f * (56 * 256), 1024, tag, v);
      f32x2 s = ((v[0] + v[1]) + (v[2] + v[3])) + ((v[4] + v[5]) + (v[6] + v[7])) + (((v[8] + v[9]) + (v[10] + v[11])) + (v[12] + v[13]));
      const float g0 = rbf(xrows_sum(s[0])), g1 = rbf(xrows_sum(s[1]));
      gate = f32x2{rbf(g0 / (1.f + __expf(-g0))), rbf(g1 / (1.f + __expf(-g1)))};
    }
    if (wave == 0) STAMP(5);
    nap(4);
    { u32 hv[3]; sweep1<3, 32>(wsr, lane * 8, WS_HINT + (kvh * 8 + t) * 2048, 512, tag, hv); }
    wg_barrier();
    if (wave == 0) STAMP(7);
    {
      f32x2 v[8];
      sweep2<8, 1>(wsr, lane * 16, WS_H2 + f * (32 * 256), 1024, tag, v);
      if (wave == 0) STAMP(23);
      const f32x2 s = ((v[0] + v[1]) + (v[2] + v[3])) + ((v[4] + v[5]) + (v[6] + v[7]));
      const f32x2 u = swap16(s[0], s[1]);
      const float h = u[0] + u[1];
      const f32x2 w = swap32(h, h);
      const float sv = rbf(w[0] + w[1]);
      const int odd = (lane >> 4) & 1;
      const float r = (odd ? xres[1] : xres[0]) + rbf(sv * (odd ? gate[1] : gate[0]));
      if (lane < 32) out_[(2 * bp + odd) * D + 16 * ct + (lane & 15)] = (u16)f2bf(r);
      if (f == 0 && lane == 0) __hip_atomic_store((GAS u32*)(ws + WS_EPOCH), tag, RLX_AGENT);
    }
    if (wave == 0) STAMP(8);
    return;
  }

  volatile LAS u32* ctl = (volatile LAS u32*)(L + L_CTL);
  const int b = jj / 3, sp = jj - 3 * b, pair = b * 8 + kvh;
  const int nu = sp == 2 ? 170 : 171;

  if (wave == 0) {
    __builtin_amdgcn_s_setprio(3);
    STAMP(0);
    const rsrc_t kr = mk_rsrc(kc_, (u32)B * HKV * SK * DH * 2), vr = mk_rsrc(vc_, (u32)B * HKV * SK * DH * 2);
    const u32 so0 = (u32)pair * (SK * DH * 2) + (u32)sp * (171 * 4096), vo = lane * 16;
    constexpr int WU = WIN / 4;
#define DMA_UNIT(R, SO, M0, PUB) asm volatile("ds_read_b32 %0, %8\n\ts_mov_b32 m0, %1\n\t" \
      "s_waitcnt vmcnt(%5)\n\tbuffer_load_dwordx4 %2, %3, %4 offen nt lds\n\t" \
      "s_waitcnt vmcnt(%5)\n\tbuffer_load_dwordx4 %2, %3, %4 offen offset:1024 nt lds\n\t" \
      "s_waitcnt vmcnt(%5)\n\tbuffer_load_dwordx4 %2, %3, %4 offen offset:2048 nt lds\n\t" \
      "s_waitcnt vmcnt(%5)\n\tbuffer_load_dwordx4 %2, %3, %4 offen offset:3072 nt lds\n\t" \
      "s_waitcnt lgkmcnt(0)\n\tds_write_b32 %6, %7" \
      : "=&v"(dn) : "s"(M0), "v"(vo), "s"(R), "s"(SO), "n"(WIN - 1), "v"(pubaddr), "v"(PUB), "v"(dnaddr) : "m0", "memory")
    u32 dn;
    const u32 dnaddr = ldsb + L_CTL + 32 + 4 * (lane & 7);
    {
      const u32 pubaddr = ldsb + L_CTL + 28;
#pragma unroll
      for (int u = 0; u < WU; ++u) DMA_UNIT(kr, so0 + u * 4096, ldsb + u * 4096, 0u);
    }
    wg_barrier();
    const u32 pubaddr = ldsb + L_CTL;
    dn = 0u;
#pragma unroll
    for (int u = WU; u < 16; ++u) DMA_UNIT(kr, so0 + u * 4096, ldsb + u * 4096, (u32)(u - WU + 1));
    u32 pub = 0, iss = 16;
    for (int g = 2; g < 2 * NGRP; ++g) {
      const int gk = g < NGRP ? g : g - NGRP, cnt = gk < NGRP - 1 ? 8 : nu - 8 * (NGRP - 1);
      const int need = g - NSLOT / 8 + 1;
      u32 so = so0 + (u32)gk * 32768, m0 = ldsb + (u32)(g & (NSLOT / 8 - 1)) * 32768;
      const rsrc_t rr = g < NGRP ? kr : vr;
#pragma unroll 1
      for (int i = 0; i < cnt; ++i) {
        if ((int)__builtin_amdgcn_readlane(dn, i) < need) {
          VM0();
          pub = iss;
          ctl[0] = pub;
          lds_wait_ge(ctl + 8 + i, (u32)need);
        }
        const u32 pv = iss - WU + 1;
        pub = pub > pv ? pub : pv;
        DMA_UNIT(rr, so, m0, pub);
        so += 4096; m0 += 4096; ++iss;
      }
    }
#pragma unroll
    for (int r = WU - 1; r >= 0; --r) {
      asm volatile("s_waitcnt vmcnt(%0)" :: "n"(4 * r) : "memory");
      pub = pub > iss - r ? pub : iss - r;
      ctl[0] = pub;
    }
    STAMP(2);
    return;
  }

  if (wave == NC + 1) {
    u32 tag = __hip_atomic_load((const GAS u32*)(ws + WS_EPOCH), RLX_AGENT);
    if (lane < 16) ctl[lane] = 0u;
    wg_barrier();
    asm volatile("" : "+v"(tag));
    tag = __builtin_amdgcn_readfirstlane(tag) + 1u;
    ctl[3] = tag;
    __builtin_amdgcn_s_sleep(H0_DELAY);
    f32x2 v[2];
    if (SKIP & 16) { v[0] = v[1] = f32x2{1e-3f, 1e-3f}; } else
    sweep2<2, 16>(wsr, lane * 16, WS_H0 + pair * 2048, 1024, tag, v);
    *(LAS f32x2*)(L + L_Q + 8 * lane) = v[0];
    *(LAS f32x2*)(L + L_Q + 512 + 8 * lane) = v[1];
    LGKM0();
    ctl[2] = 1u;
    STAMP(1);
    return;
  }

  const int c = wave - 1, cl = lane & 31;
  wg_barrier();
  const LAS unsigned char* rp = L + L_RING + c * 4096 + lane * 16;
#define TAKE(dst, g, need) do { lds_wait_ge(ctl, (u32)(need)); \
    _Pragma("unroll") for (int i_ = 0; i_ < 4; ++i_) dst[i_] = *(const LAS u32x4*)(rp + ((g) & 3) * 32768 + i_ * 1024); \
    LGKM0(); ctl[8 + c] = (u32)((g) + 1); PIN4(dst); } while (0)
  LAS float* scp = (LAS float*)(L + L_SC + c * 1408);
  LAS float* scw = scp + (lane >> 5) * 8 + ((lane >> 2) & 3) * 2 + ((lane >> 4) & 1);
  const bool last = 8 * (NGRP - 1) + c < nu;
  u32x4 q0, q1;
  float m;
  int g0 = 0;
  {
    u32x4 k0[4], k1[4], k2[4], k3[4], k4[4], k5[4], k6[4], k7[4];
#define PRE(kk, i) if (g0 == (i) && __builtin_amdgcn_readfirstlane(ctl[2]) == 0u) { TAKE(kk, i, c + 8 * (i) + 1); g0 = (i) + 1; }
    PRE(k0, 0) PRE(k1, 1) PRE(k2, 2) PRE(k3, 3) PRE(k4, 4) PRE(k5, 5) PRE(k6, 6) PRE(k7, 7)
    lds_wait_ge(ctl + 2, 1u);
    q0 = *(const LAS u32x4*)(L + L_Q + 16 * cl); q1 = *(const LAS u32x4*)(L + L_Q + 512 + 16 * cl);
    scw[16 * (NGRP - 1)] = -INFINITY;
    m = -INFINITY;
#define POST(kk, i) if (g0 > (i)) { const float s_ = k_unit(kk, q0, q1); scw[16 * (i)] = s_; m = fmaxf(m, s_); }
    POST(k0, 0) POST(k1, 1) POST(k2, 2) POST(k3, 3) POST(k4, 4) POST(k5, 5) POST(k6, 6) POST(k7, 7)
  }
  const int ng = last ? NGRP : NGRP - 1;
#pragma unroll 1
  for (int g = g0; g < ng; ++g) {
    u32x4 k[4];
    TAKE(k, g, 8 * g + c + 1);
    const float s = k_unit(k, q0, q1);
    scw[16 * g] = s;
    m = fmaxf(m, s);
  }
  ctl[8 + c] = (u32)NGRP;
  {
    m = fmaxf(m, dpp<ROR4>(m)); m = fmaxf(m, dpp<ROR8>(m));
    { const f32x2 t = swap32(m, m); m = fmaxf(t[0], t[1]); }
    if ((lane & 47) == 0) ((LAS float*)(L + L_MX))[2 * c + (lane >> 4)] = m;
    LGKM0();
    if (lane == 0) __hip_atomic_fetch_add((LAS u32*)(L + L_CTL + 4), 1u, __ATOMIC_RELAXED, __HIP_MEMORY_SCOPE_WORKGROUP);
    lds_wait_ge(ctl + 1, 8u);
    float M = ((const LAS float*)(L + L_MX))[lane & 15];
    M = fmaxf(M, dpp<XOR2>(M)); M = fmaxf(M, dpp<ROR4>(M)); M = fmaxf(M, dpp<ROR8>(M));
    const float M0 = dpp<0xA0>(M), M1 = dpp<0xF5>(M);
    float l0 = 0.f, l1 = 0.f;
#pragma unroll
    for (int i = 0; i < 2; ++i) {
      if (i == 0 || lane < 24) {
        f32x4 s = *(const LAS f32x4*)(scp + 4 * (lane + 64 * i));
        s[0] = __builtin_amdgcn_exp2f((s[0] - M0) * CEXP); s[1] = __builtin_amdgcn_exp2f((s[1] - M1) * CEXP);
        s[2] = __builtin_amdgcn_exp2f((s[2] - M0) * CEXP); s[3] = __builtin_amdgcn_exp2f((s[3] - M1) * CEXP);
        *(LAS f32x4*)(scp + 4 * (lane + 64 * i)) = s;
        l0 += s[0] + s[2]; l1 += s[1] + s[3];
      }
    }
    l0 = wave_sum(l0); l1 = wave_sum(l1);
    if (lane == 0) *(LAS f32x2*)(L + L_MX + 64 + 8 * c) = f32x2{l0, l1};
    LGKM0();
    u32 old = 0;
    if (lane == 0) old = __hip_atomic_fetch_add((LAS u32*)(L + L_CTL + 16), 1u, __ATOMIC_RELAXED, __HIP_MEMORY_SCOPE_WORKGROUP);
    old = __builtin_amdgcn_readfirstlane(old);
    asm volatile("" ::: "memory");
    if (old == 7u) {
      float Ls = ((const LAS float*)(L + L_MX + 64))[lane & 15];
      Ls += dpp<XOR2>(Ls); Ls += dpp<ROR4>(Ls); Ls += dpp<ROR8>(Ls);
      if (lane < 4) st_gran((GAS u64*)(ws + WS_ML) + pair * 16 + sp * 4 + lane, __builtin_amdgcn_readfirstlane(ctl[3]), __float_as_uint((lane & 2) ? Ls : M));
    }
  }
  f32x2 acc[8];
#pragma unroll
  for (int j = 0; j < 8; ++j) acc[j] = f32x2{0.f, 0.f};
  const LAS float* scr = scp + (lane >> 5) * 8;
  const u32 tag = __builtin_amdgcn_readfirstlane(ctl[3]);
#pragma unroll 1
  for (int g = 0; g < ng; ++g) {
    const f32x4 P0 = *(const LAS f32x4*)(scr + 16 * g), P1 = *(const LAS f32x4*)(scr + 16 * g + 4);
    u32x4 v[4];
    TAKE(v, NGRP + g, nu + 8 * g + c + 1);
    if (c == HINT_C && g == HINT_G) st_gran((GAS u64*)(ws + WS_HINT) + lane * 256 + kvh * 24 + jj, tag, 1u);
    v_piece(acc, P0[0], P0[1], v[0]); v_piece(acc, P0[2], P0[3], v[1]);
    v_piece(acc, P1[0], P1[1], v[2]); v_piece(acc, P1[2], P1[3], v[3]);
  }
  if (c == 1) STAMP(3);
  {
    float o[8];
#pragma unroll
    for (int j = 0; j < 8; ++j) { const f32x2 t = swap32(acc[j >> 1][j & 1], acc[4 + (j >> 1)][j & 1]); o[j] = t[0] + t[1]; }
    LAS float* dst = (LAS float*)(L + L_RING + c * 4096) + (lane >> 5) * 256 + 8 * cl;
    *(LAS f32x4*)dst = f32x4{o[0], o[1], o[2], o[3]};
    *(LAS f32x4*)(dst + 4) = f32x4{o[4], o[5], o[6], o[7]};
  }
  wg_barrier();
  if (c < 4) {
    const LAS f32x2* pp = (const LAS f32x2*)(L + L_RING) + c * 64 + lane;
    const f32x2 o = ((pp[0] + pp[512]) + (pp[1024] + pp[1536])) + ((pp[2048] + pp[2560]) + (pp[3072] + pp[3584]));
    st_gran2(wsr, lane * 16, WS_H1 + (((4 * kvh + c) * 8 + b) * 3 + sp) * 1024, tag, o[0], o[1]);
  }
  if (c == 1) STAMP(4);
}

extern "C" void kernel_launch(void* const* d_in, const int* in_sizes, int n_in, void* d_out, int out_size, void* d_ws,
                              size_t ws_size, hipStream_t stream) {
  (void)hipFuncSetAttribute((const void*)fused_decode, hipFuncAttributeMaxDynamicSharedMemorySize, L_BYTES);
  fused_decode<<<256, NTHR, L_BYTES, stream>>>((const u16*)d_in[0], (const u16*)d_in[1], (const u16*)d_in[2], (const u16*)d_in[3],
                                               (const u16*)d_in[4], (const u16*)d_in[6], (const u16*)d_in[7], (u16*)d_out,
                                               (unsigned char*)d_ws);
}
```

```cpp
#define POLK "nt"
#define POLV "nt"
#include <hip/hip_runtime.h>
#include <hip/hip_bf16.h>
#include <stdint.h>

typedef unsigned u32;
typedef unsigned long long u64;
typedef unsigned short u16;
typedef unsigned u32x4 __attribute__((ext_vector_type(4)));
typedef unsigned u32x2 __attribute__((ext_vector_type(2)));
typedef float f32x4 __attribute__((ext_vector_type(4)));
typedef float f32x2 __attribute__((ext_vector_type(2)));
typedef short s16x4 __attribute__((ext_vector_type(4)));
typedef short s16x8 __attribute__((ext_vector_type(8)));
typedef __attribute__((ext_vector_type(2))) __bf16 bf16x2;
typedef __attribute__((ext_vector_type(8))) __bf16 bf16x8;
#define GAS __attribute__((address_space(1)))
#define LAS __attribute__((address_space(3)))
#define RLX_AGENT __ATOMIC_RELAXED, __HIP_MEMORY_SCOPE_AGENT

#define WIN 44
#define WAUX 2
#define H0_DELAY 100
#define HINT_G 20
#define HINT_C 0
#define SKIP 0
#define STAMPS 0

constexpr int B = 8, D = 448, HQ = 16, HKV = 8, SK = 4096, DH = 256, DFF = 1792;
constexpr int NC = 8, NTHR = (NC + 2) * 64;
constexpr int NSLOT = 32, NGRP = 22;
constexpr float CEXP = 0.0625f * 1.4426950408889634f;
constexpr unsigned SPIN_CAP = 1u << 18;

constexpr u32 WS_EPOCH = 0;
constexpr u32 WS_H0 = 65536;
constexpr u32 WS_ML = WS_H0 + 64 * 256 * 8;
constexpr u32 WS_HINT = WS_ML + 64 * 16 * 8;
constexpr u32 WS_H1 = WS_HINT + 64 * 256 * 8;
constexpr u32 WS_HF = WS_H1 + 32 * 8 * 3 * 128 * 8;
constexpr u32 WS_H2 = WS_HF + 4 * 28 * 56 * 32 * 8;
constexpr u32 WS_ST = WS_H2 + 4 * 28 * 32 * 32 * 8;

constexpr int L_RING = 0;
constexpr int L_Q = NSLOT * 4096;
constexpr int L_SC = L_Q + 1024;
constexpr int L_MX = L_SC + 8 * 1408;
constexpr int L_CTL = L_MX + 128;
constexpr int XN_STRIDE = 912, HA_STRIDE = 80, OA_STRIDE = 272;
constexpr int S_WQ = 0;
constexpr int S_W1 = S_WQ + 57344;
constexpr int S_W2 = S_W1 + 28672;
constexpr int S_XN = S_W2 + 28672;
constexpr int S_HA = S_XN + 8 * XN_STRIDE;
constexpr int S_OA = S_HA + 8 * HA_STRIDE;
constexpr int S_TAG = S_OA + 8 * OA_STRIDE;
constexpr int L_BYTES = L_CTL + 64 + 128;
static_assert(L_BYTES <= 160 * 1024 && S_TAG + 16 <= L_BYTES, "LDS");

__device__ __forceinline__ float bf2f(u32 b) { return __uint_as_float(b << 16); }
__device__ __forceinline__ u32 f2bf(float f) { __bf16 b = (__bf16)f; return (u32)__builtin_bit_cast(u16, b); }
__device__ __forceinline__ float rbf(float f) { return bf2f(f2bf(f)); }
__device__ __forceinline__ u32 pk2(float lo, float hi) {
  bf16x2 v = {(__bf16)lo, (__bf16)hi};
  return __builtin_bit_cast(u32, v);
}
__device__ __forceinline__ float dot2(u32 a, u32 b, float acc) {
  return __builtin_amdgcn_fdot2_f32_bf16(__builtin_bit_cast(bf16x2, a), __builtin_bit_cast(bf16x2, b), acc, false);
}
template <int CTRL> __device__ __forceinline__ float dpp(float x) {
  return __builtin_bit_cast(float, __builtin_amdgcn_mov_dpp(__builtin_bit_cast(int, x), CTRL, 0xf, 0xf, true));
}
template <int CTRL, int BANK> __device__ __forceinline__ float dppm(float old, float x) {
  return __builtin_bit_cast(float, __builtin_amdgcn_update_dpp(__builtin_bit_cast(int, old), __builtin_bit_cast(int, x), CTRL, 0xf, BANK, false));
}
constexpr int XOR1 = 0xB1, XOR2 = 0x4E, ROR4 = 0x124, ROR8 = 0x128, SHL4 = 0x104, SHR4 = 0x114, BCAST = 0x150;
__device__ __forceinline__ f32x2 swap16(float a, float b) {
  auto s = __builtin_amdgcn_permlane16_swap(__float_as_uint(a), __float_as_uint(b), false, false);
  return f32x2{__uint_as_float(s[0]), __uint_as_float(s[1])};
}
__device__ __forceinline__ f32x2 swap32(float a, float b) {
  auto s = __builtin_amdgcn_permlane32_swap(__float_as_uint(a), __float_as_uint(b), false, false);
  return f32x2{__uint_as_float(s[0]), __uint_as_float(s[1])};
}
__device__ __forceinline__ float xrows_sum(float x) {
  f32x2 s = swap16(x, x); x = s[0] + s[1];
  f32x2 t = swap32(x, x); return t[0] + t[1];
}
__device__ __forceinline__ float wave_sum(float x) {
  x += dpp<XOR1>(x); x += dpp<XOR2>(x); x += dpp<ROR4>(x); x += dpp<ROR8>(x);
  return xrows_sum(x);
}
typedef __amdgpu_buffer_rsrc_t rsrc_t;
__device__ __forceinline__ rsrc_t mk_rsrc(const void* p, u32 bytes) { return __builtin_amdgcn_make_buffer_rsrc((void*)p, 0, (int)bytes, 0x00020000); }
__device__ __forceinline__ u32x4 ldw(rsrc_t r, u32 voff, u32 soff) { return __builtin_amdgcn_raw_buffer_load_b128(r, voff, soff, WAUX); }
#define LGKM0() asm volatile("s_waitcnt lgkmcnt(0)" ::: "memory")
__device__ __forceinline__ void wg_barrier() {
  LGKM0();
  __builtin_amdgcn_s_barrier();
  asm volatile("" ::: "memory");
}
__device__ __forceinline__ void st_gran(GAS u64* g, u32 tag, u32 v) { __hip_atomic_store(g, ((u64)tag << 32) | (u64)v, RLX_AGENT); }
__device__ __forceinline__ u64 ld_gran(const GAS u64* g) { return __hip_atomic_load(g, RLX_AGENT); }
#define PSLEEP 1
__device__ __forceinline__ void lds_wait_ge(volatile LAS u32* p, u32 need) {
  for (unsigned spins = 0; (u32)__builtin_amdgcn_readfirstlane(*p) < need && spins < (SPIN_CAP << 4); ++spins) __builtin_amdgcn_s_sleep(PSLEEP);
  asm volatile("" ::: "memory");
}
#define PIN8(a) asm volatile("" : "+v"(a[0]), "+v"(a[1]), "+v"(a[2]), "+v"(a[3]), "+v"(a[4]), "+v"(a[5]), "+v"(a[6]), "+v"(a[7]))
#define PIN4(a) asm volatile("" : "+v"(a[0]), "+v"(a[1]), "+v"(a[2]), "+v"(a[3]) : : "memory")

#define STAMP(i) do { } while (0)

__device__ __forceinline__ float k_unit(const u32x4 (&k)[4], const u32x4& q0, const u32x4& q1) {
  float e[4];
#pragma unroll
  for (int i = 0; i < 4; ++i) {
    float a = 0.f, b = 0.f;
#pragma unroll
    for (int j = 0; j < 4; ++j) { a = dot2(k[i][j], q0[j], a); b = dot2(k[i][j], q1[j], b); }
    const f32x2 s = swap16(a, b);
    e[i] = s[0] + s[1];
  }
  const float f0 = dppm<ROR8, 0x3>(e[2], e[0]) + dppm<ROR8, 0xc>(e[0], e[2]);
  const float f1 = dppm<ROR8, 0x3>(e[3], e[1]) + dppm<ROR8, 0xc>(e[1], e[3]);
  float g = dppm<SHL4, 0x5>(f1, f0) + dppm<SHR4, 0xa>(f0, f1);
  g += dpp<XOR1>(g); g += dpp<XOR2>(g);
  return rbf(g);
}
__device__ __forceinline__ void v_piece(f32x2 (&acc)[8], float p0, float p1, u32x4 v) {
#pragma unroll
  for (int j = 0; j < 4; ++j) {
    const f32x2 vv = {__uint_as_float(v[j] << 16), __uint_as_float(v[j] & 0xffff0000u)};
    acc[j] = __builtin_elementwise_fma(vv, f32x2{p0, p0}, acc[j]);
    acc[4 + j] = __builtin_elementwise_fma(vv, f32x2{p1, p1}, acc[4 + j]);
  }
}

#define DMA_W(R, VO, SO, M0) asm volatile("s_mov_b32 m0, %0\n\ts_nop 0\n\tbuffer_load_dwordx4 %1, %2, %3 offen nt lds" :: "s"(M0), "v"(VO), "s"(R), "s"(SO) : "m0", "memory")
#define VM0() asm volatile("s_waitcnt vmcnt(0)" ::: "memory")
__device__ __forceinline__ s16x8 tr_frag(const LAS unsigned char* p, int four_rows) {
  const s16x4 b0 = __builtin_amdgcn_ds_read_tr16_b64_v4i16((LAS s16x4*)p);
  const s16x4 b1 = __builtin_amdgcn_ds_read_tr16_b64_v4i16((LAS s16x4*)(p + four_rows));
  return s16x8{b0[0], b0[1], b0[2], b0[3], b1[0], b1[1], b1[2], b1[3]};
}
__device__ __forceinline__ f32x4 mfma16(s16x8 a, s16x8 b, f32x4 c) {
  return __builtin_amdgcn_mfma_f32_16x16x32_bf16(__builtin_bit_cast(bf16x8, a), __builtin_bit_cast(bf16x8, b), c, 0, 0, 0);
}
template <int KS> __device__ __forceinline__ f32x4 gemm8x16(const LAS unsigned char* A, int astride, const LAS unsigned char* Bt, int bstride, int lane) {
  f32x4 acc = {0.f, 0.f, 0.f, 0.f};
  const int g = lane >> 4;
  const LAS unsigned char* ap = A + (lane & 7) * astride + 16 * g;
  const LAS unsigned char* bp = Bt + (8 * g + ((lane & 15) >> 2)) * bstride + 8 * (lane & 3);
#pragma unroll
  for (int ks = 0; ks < KS; ++ks) acc = mfma16(*(const LAS s16x8*)(ap + 64 * ks), tr_frag(bp + ks * 32 * bstride, 4 * bstride), acc);
  return acc;
}
__device__ __forceinline__ u32 pair_of(f32x4 acc, int lane, int& batch) {
  const int g = lane >> 4;
  const float lo = (g >> 1) ? acc[2] : acc[0], hi = (g >> 1) ? acc[3] : acc[1];
  const float send = (lane & 1) ? lo : hi, keep = (lane & 1) ? hi : lo;
  const float got = dpp<XOR1>(send);
  batch = 4 * (g & 1) + (g >> 1) * 2 + (lane & 1);
  return (lane & 1) ? pk2(got, keep) : pk2(keep, got);
}
__device__ __forceinline__ void st_gran2(rsrc_t wsr, u32 voff, u32 soff, u32 tag, float a, float b) {
  __builtin_amdgcn_raw_buffer_store_b128(u32x4{__float_as_uint(a), tag, __float_as_uint(b), tag}, wsr, voff + soff, 0, 16);
}
__device__ __forceinline__ void pub_tile(rsrc_t wsr, u32 soff, u32 pstride, f32x4 acc, u32 tag, int lane) {
  const int g = lane >> 4;
  st_gran2(wsr, (u32)(2 * (g & 1) + (g >> 1)) * pstride + (lane & 15) * 16, soff, tag, (g >> 1) ? acc[2] : acc[0], (g >> 1) ? acc[3] : acc[1]);
}
__device__ __forceinline__ void nap(int units) {
  for (int i = 0; i < units; ++i) __builtin_amdgcn_s_sleep(127);
}
template <int N, int SLEEP> __device__ __forceinline__ void sweep1(rsrc_t wsr, u32 voff, u32 soff, u32 stride, u32 tag, u32 (&v)[N]) {
  unsigned have = 0;
#pragma unroll
  for (int i = 0; i < N; ++i) v[i] = 0u;
  for (unsigned spins = 0;; ++spins) {
    u32x2 t[N];
    asm volatile("" ::: "memory");
#pragma unroll
    for (int i = 0; i < N; ++i) t[i] = __builtin_amdgcn_raw_buffer_load_b64(wsr, voff, ((have >> i) & 1u) ? 0x40000000u : soff + i * stride, 16);
#pragma unroll
    for (int i = 0; i < N; ++i) {
      v[i] = ((have >> i) & 1u) ? v[i] : t[i][0];
      have |= __all(t[i][1] == tag) ? 1u << i : 0u;
    }
    if (have == (1u << N) - 1u || spins > SPIN_CAP) break;
    __builtin_amdgcn_s_sleep(SLEEP);
  }
  asm volatile("" ::: "memory");
}
template <int N, int SLEEP> __device__ __forceinline__ void sweep2(rsrc_t wsr, u32 voff, u32 soff, u32 stride, u32 tag, f32x2 (&v)[N]) {
  unsigned have = 0;
#pragma unroll
  for (int i = 0; i < N; ++i) v[i] = f32x2{0.f, 0.f};
  for (unsigned spins = 0;; ++spins) {
    u32x4 t[N];
    asm volatile("" ::: "memory");
#pragma unroll
    for (int i = 0; i < N; ++i) t[i] = __builtin_amdgcn_raw_buffer_load_b128(wsr, voff, ((have >> i) & 1u) ? 0x40000000u : soff + i * stride, 16);
#pragma unroll
    for (int i = 0; i < N; ++i) {
      v[i] = ((have >> i) & 1u) ? v[i] : f32x2{__uint_as_float(t[i][0]), __uint_as_float(t[i][2])};
      have |= __all(t[i][1] == tag && t[i][3] == tag) ? 1u << i : 0u;
    }
    if (have == (1u << N) - 1u || spins > SPIN_CAP) break;
    __builtin_amdgcn_s_sleep(SLEEP);
  }
  asm volatile("" ::: "memory");
}

__global__ void __launch_bounds__(NTHR, 3)
fused_decode(const u16* __restrict__ x_, const u16* __restrict__ kc_, const u16* __restrict__ vc_, const u16* __restrict__ wq_,
             const u16* __restrict__ wo_, const u16* __restrict__ w1_, const u16* __restrict__ w2_, u16* __restrict__ out_,
             unsigned char* ws_) {
  extern __shared__ __attribute__((aligned(16))) unsigned char smem[];
  LAS unsigned char* L = (LAS unsigned char*)smem;
  GAS unsigned char* ws = (GAS unsigned char*)ws_;
  const int tid = threadIdx.x, lane = tid & 63, wave = __builtin_amdgcn_readfirstlane(tid >> 6);
  const int bid = blockIdx.x, kvh = bid & 7, jj = bid >> 3;
  const u32 ldsb = (u32)(uintptr_t)L;
  const rsrc_t wsr = mk_rsrc(ws_, WS_ST);

  if (jj >= 24) {
    const int t = jj - 24, c = wave - 1;
    const bool mid = wave >= 1 && wave <= 8;
    if (wave == 0) STAMP(0);
    u32 tag;
    if (mid) {
      u32x4 xw;
      asm volatile("global_load_dwordx4 %0, %1, off" : "=v"(xw) : "v"(x_ + c * D + 8 * (lane < 56 ? lane : 55)) : "memory");
      {
        const rsrc_t r = mk_rsrc(wq_, (SKIP & 8) ? 0u : (u32)D * HQ * DH * 2);
        const u32 vo = (lane >> 3) * (HQ * DH * 2) + (lane & 7) * 16;
#pragma unroll
        for (int it = 0; it < 7; ++it) DMA_W(r, vo, (56 * c + 8 * it) * (HQ * DH * 2) + kvh * 1024 + t * 128, ldsb + S_WQ + (7 * c + it) * 1024);
      }
      asm volatile("s_waitcnt vmcnt(7)" : "+v"(xw) :: "memory");
      float xv[8];
#pragma unroll
      for (int j = 0; j < 4; ++j) { xv[2 * j] = __uint_as_float(xw[j] << 16); xv[2 * j + 1] = __uint_as_float(xw[j] & 0xffff0000u); }
      float s = ((xv[0] + xv[1]) + (xv[2] + xv[3])) + ((xv[4] + xv[5]) + (xv[6] + xv[7]));
      const float mean = wave_sum(lane < 56 ? s : 0.f) * (1.f / D);
      float ss = 0.f;
#pragma unroll
      for (int j = 0; j < 8; ++j) { const float d = xv[j] - mean; ss = fmaf(d, d, ss); }
      const float var = rbf(wave_sum(lane < 56 ? ss : 0.f) * (1.f / D));
      const float mb = rbf(mean), sd = rbf(sqrtf(rbf(var + 1e-5f)));
      u32x4 o;
#pragma unroll
      for (int j = 0; j < 4; ++j) o[j] = pk2(rbf(xv[2 * j] - mb) / sd, rbf(xv[2 * j + 1] - mb) / sd);
      if (lane < 56) *(LAS u32x4*)(L + S_XN + c * XN_STRIDE + 16 * lane) = o;
      VM0();
    } else {
      if (wave == 0) {
        const u32 e = __hip_atomic_load((const GAS u32*)(ws + WS_EPOCH), RLX_AGENT);
        if (lane == 0) *(LAS u32*)(L + S_TAG) = e + 1u;
      }
    }
    wg_barrier();
    tag = __builtin_amdgcn_readfirstlane(*(const LAS u32*)(L + S_TAG));
    if (wave >= 1 && wave <= 4) {
      const f32x4 acc = gemm8x16<14>(L + S_XN, XN_STRIDE, L + S_WQ + 32 * c, 128, lane);
      int bb; const u32 v = pair_of(acc, lane, bb);
      st_gran((GAS u64*)(ws + WS_H0) + (size_t)(bb * 8 + kvh) * 256 + 32 * t + 8 * c + ((lane & 15) >> 1), tag, v);
      if (wave == 1) STAMP(9);
    } else if (t < 7) {
      const int w = wave == 0 ? 0 : wave - 4;
      const rsrc_t r1 = mk_rsrc(w1_, (u32)D * DFF * 2), r2 = mk_rsrc(w2_, (u32)D * DFF * 2);
      const u32 vo = (lane >> 2) * (DFF * 2) + (lane & 3) * 16;
      for (int it = w; it < ((SKIP & 1) ? 0 : 28); it += 6) DMA_W(r1, vo, it * 16 * (DFF * 2) + kvh * 448 + t * 64, ldsb + S_W1 + it * 1024);
      for (int it = w; it < ((SKIP & 1) ? 0 : 28); it += 6) DMA_W(r2, (u32)lane * 16, (kvh * 224 + t * 32) * (D * 2) + it * 1024, ldsb + S_W2 + it * 1024);
      VM0();
    }
    wg_barrier();
    if (mid) {
      const rsrc_t r = mk_rsrc(wo_, (u32)D * HQ * DH * 2);
#pragma unroll
      for (int it = 0; it < ((SKIP & 4) ? 0 : 7); ++it) {
        const int i = lane + 64 * (7 * c + it), row = i / 28;
        DMA_W(r, (u32)(row * (D * 2) + (i - row * 28) * 16), (4 * kvh + (t >> 1)) * 128 * (D * 2) + (t & 1) * 448, ldsb + S_WQ + (7 * c + it) * 1024);
      }
    } else if (t < 7) {
      const int half = wave != 0;
      f32x4 acc = gemm8x16<14>(L + S_XN, XN_STRIDE, L + S_W1 + 32 * half, 64, lane);
#pragma unroll
      for (int i = 0; i < 4; ++i) acc[i] = fmaxf(acc[i], 0.f);
      int bb; const u32 v = pair_of(acc, lane, bb);
      *(LAS u32*)(L + S_HA + bb * HA_STRIDE + half * 32 + 4 * ((lane & 15) >> 1)) = v;
    }
    wg_barrier();
    if (t < 7 && wave >= 1 && wave <= 7 && !(SKIP & 2)) {
#pragma unroll
      for (int i = 0; i < 4; ++i) {
        const f32x4 acc = gemm8x16<1>(L + S_HA, HA_STRIDE, L + S_W2 + 32 * (4 * c + i), D * 2, lane);
        pub_tile(wsr, WS_HF + ((4 * c + i) * 56 + kvh * 7 + t) * 256, 28 * 56 * 256, acc, tag, lane);
      }
    }
    VM0();
    wg_barrier();
    if (mid) {
      s16x8 wb[2][4];
      {
        const LAS unsigned char* bp = L + S_WQ + (8 * (lane >> 4) + ((lane & 15) >> 2)) * 448 + 64 * (c < 7 ? c : 6) + 8 * (lane & 3);
#pragma unroll
        for (int i = 0; i < 2; ++i)
#pragma unroll
          for (int ks = 0; ks < 4; ++ks) wb[i][ks] = tr_frag(bp + 32 * i + ks * 32 * 448, 4 * 448);
        asm volatile("" : "+v"(wb[0][0]), "+v"(wb[0][1]), "+v"(wb[0][2]), "+v"(wb[0][3]), "+v"(wb[1][0]), "+v"(wb[1][1]), "+v"(wb[1][2]), "+v"(wb[1][3]));
      }
      float w0, w1, w2;
      {
        nap(5);
        u32 v[1];
        sweep1<1, 100>(wsr, (u32)((lane & 15) < 12 ? (lane & 15) : 0) * 8, WS_ML + (c * 8 + kvh) * 128, 0, tag, v);
        const int h = t >> 2;
        float mv[3], lv[3];
#pragma unroll
        for (int s = 0; s < 3; ++s) {
          mv[s] = __uint_as_float(__builtin_amdgcn_readlane(v[0], 4 * s + h));
          lv[s] = __uint_as_float(__builtin_amdgcn_readlane(v[0], 4 * s + 2 + h));
        }
        const float M = fmaxf(fmaxf(mv[0], mv[1]), mv[2]);
        w0 = __builtin_amdgcn_exp2f((mv[0] - M) * CEXP); w1 = __builtin_amdgcn_exp2f((mv[1] - M) * CEXP); w2 = __builtin_amdgcn_exp2f((mv[2] - M) * CEXP);
        const float Ls = fmaf(w0, lv[0], fmaf(w1, lv[1], w2 * lv[2]));
        w0 /= Ls; w1 /= Ls; w2 /= Ls;
      }
      if (c == 0) STAMP(10);
      {
        u32 hv[1];
        sweep1<1, 16>(wsr, (u32)((lane & 3) < 3 ? (lane & 3) : 0) * 8, WS_HINT + (kvh * 8 + t) * 2048 + (kvh * 24 + c * 3) * 8, 0, tag, hv);
        f32x2 v[3];
        sweep2<3, 1>(wsr, lane * 16, WS_H1 + ((4 * kvh + (t >> 1)) * 8 + c) * 3072, 1024, tag, v);
        *(LAS u32*)(L + S_OA + c * OA_STRIDE + 4 * lane) =
            pk2(fmaf(w0, v[0][0], fmaf(w1, v[1][0], w2 * v[2][0])), fmaf(w0, v[0][1], fmaf(w1, v[1][1], w2 * v[2][1])));
      }
      if (c == 7) STAMP(6);
      wg_barrier();
      if (c < 7) {
        const LAS unsigned char* ap = L + S_OA + (lane & 7) * OA_STRIDE + 16 * (lane >> 4);
        f32x4 s0 = {0.f, 0.f, 0.f, 0.f}, s1 = {0.f, 0.f, 0.f, 0.f};
#pragma unroll
        for (int ks = 0; ks < 4; ++ks) { const s16x8 a = *(const LAS s16x8*)(ap + 64 * ks); s0 = mfma16(a, wb[0][ks], s0); s1 = mfma16(a, wb[1][ks], s1); }
        const u32 so = WS_H2 + ((14 * (t & 1) + 2 * c) * 32 + 4 * kvh + (t >> 1)) * 256;
        pub_tile(wsr, so, 28 * 32 * 256, s0, tag, lane);
        pub_tile(wsr, so + 32 * 256, 28 * 32 * 256, s1, tag, lane);
        if (c == 0) STAMP(21);
        if (STAMPS) { VM0(); if (c == 0) STAMP(22); }
      }
      return;
    }
    if (t == 7) return;
    const int f = 2 * (7 * kvh + t) + (wave != 0), bp = f / 28, ct = f - bp * 28;
    f32x2 gate, xres;
    {
      const GAS u16* xp = (const GAS u16*)x_ + 2 * bp * D + 16 * ct + (lane & 15);
      xres = f32x2{bf2f(xp[0]), bf2f(xp[D])};
      nap(1);
      f32x2 v[14];
      if (SKIP & 2) { for (int i = 0; i < 14; ++i) v[i] = xres; } else
      sweep2<14, 100>(wsr, lane * 16, WS_HF + f * (56 * 256), 1024, tag, v);
      f32x2 s = ((v[0] + v[1]) + (v[2] + v[3])) + ((v[4] + v[5]) + (v[6] + v[7])) + (((v[8] + v[9]) + (v[10] + v[11])) + (v[12] + v[13]));
      const float g0 = rbf(xrows_sum(s[0])), g1 = rbf(xrows_sum(s[1]));
      gate = f32x2{rbf(g0 / (1.f + __expf(-g0))), rbf(g1 / (1.f + __expf(-g1)))};
    }
    if (wave == 0) STAMP(5);
    nap(4);
    { u32 hv[3]; sweep1<3, 32>(wsr, lane * 8, WS_HINT + (kvh * 8 + t) * 2048, 512, tag, hv); }
    wg_barrier();
    if (wave == 0) STAMP(7);
    {
      f32x2 v[8];
      sweep2<8, 1>(wsr, lane * 16, WS_H2 + f * (32 * 256), 1024, tag, v);
      if (wave == 0) STAMP(23);
      const f32x2 s = ((v[0] + v[1]) + (v[2] + v[3])) + ((v[4] + v[5]) + (v[6] + v[7]));
      const f32x2 u = swap16(s[0], s[1]);
      const float h = u[0] + u[1];
      const f32x2 w = swap32(h, h);
      const float sv = rbf(w[0] + w[1]);
      const int odd = (lane >> 4) & 1;
      const float r = (odd ? xres[1] : xres[0]) + rbf(sv * (odd ? gate[1] : gate[0]));
      if (lane < 32) out_[(2 * bp + odd) * D + 16 * ct + (lane & 15)] = (u16)f2bf(r);
      if (f == 0 && lane == 0) __hip_atomic_store((GAS u32*)(ws + WS_EPOCH), tag, RLX_AGENT);
    }
    if (wave == 0) STAMP(8);
    return;
  }

  volatile LAS u32* ctl = (volatile LAS u32*)(L + L_CTL);
  const int b = jj / 3, sp = jj - 3 * b, pair = b * 8 + kvh;
  const int nu = sp == 2 ? 170 : 171;

  if (wave == 0) {
    __builtin_amdgcn_s_setprio(3);
    STAMP(0);
    const rsrc_t kr = mk_rsrc(kc_, (u32)B * HKV * SK * DH * 2), vr = mk_rsrc(vc_, (u32)B * HKV * SK * DH * 2);
    const u32 so0 = (u32)pair * (SK * DH * 2) + (u32)sp * (171 * 4096), vo = lane * 16;
    constexpr int WU = WIN / 4;
#define DMA_UNIT_P(POL, R, SO, M0, PUB) asm volatile("ds_read_b32 %0, %8\n\ts_mov_b32 m0, %1\n\t" \
      "s_waitcnt vmcnt(%5)\n\tbuffer_load_dwordx4 %2, %3, %4 offen " POL " lds\n\t" \
      "s_waitcnt vmcnt(%5)\n\tbuffer_load_dwordx4 %2, %3, %4 offen offset:1024 " POL " lds\n\t" \
      "s_waitcnt vmcnt(%5)\n\tbuffer_load_dwordx4 %2, %3, %4 offen offset:2048 " POL " lds\n\t" \
      "s_waitcnt vmcnt(%5)\n\tbuffer_load_dwordx4 %2, %3, %4 offen offset:3072 " POL " lds\n\t" \
      "s_waitcnt lgkmcnt(0)\n\tds_write_b32 %6, %7" \
      : "=&v"(dn) : "s"(M0), "v"(vo), "s"(R), "s"(SO), "n"(WIN - 1), "v"(pubaddr), "v"(PUB), "v"(dnaddr) : "m0", "memory")
#define DMA_UNIT(R, SO, M0, PUB) DMA_UNIT_P(POLK, R, SO, M0, PUB)
    u32 dn;
    const u32 dnaddr = ldsb + L_CTL + 32 + 4 * (lane & 7);
    {
      const u32 pubaddr = ldsb + L_CTL + 28;
#pragma unroll
      for (int u = 0; u < WU; ++u) DMA_UNIT(kr, so0 + u * 4096, ldsb + u * 4096, 0u);
    }
    wg_barrier();
    const u32 pubaddr = ldsb + L_CTL;
    dn = 0u;
#pragma unroll
    for (int u = WU; u < 16; ++u) DMA_UNIT(kr, so0 + u * 4096, ldsb + u * 4096, (u32)(u - WU + 1));
    u32 pub = 0, iss = 16;
    for (int g = 2; g < 2 * NGRP; ++g) {
      const int gk = g < NGRP ? g : g - NGRP, cnt = gk < NGRP - 1 ? 8 : nu - 8 * (NGRP - 1);
      const int need = g - NSLOT / 8 + 1;
      u32 so = so0 + (u32)gk * 32768, m0 = ldsb + (u32)(g & (NSLOT / 8 - 1)) * 32768;
      const rsrc_t rr = g < NGRP ? kr : vr;
#pragma unroll 1
      for (int i = 0; i < cnt; ++i) {
        if ((int)__builtin_amdgcn_readlane(dn, i) < need) {
          VM0();
          pub = iss;
          ctl[0] = pub;
          lds_wait_ge(ctl + 8 + i, (u32)need);
        }
        const u32 pv = iss - WU + 1;
        pub = pub > pv ? pub : pv;
        if (g < NGRP) DMA_UNIT_P(POLK, rr, so, m0, pub); else DMA_UNIT_P(POLV, rr, so, m0, pub);
        so += 4096; m0 += 4096; ++iss;
      }
    }
#pragma unroll
    for (int r = WU - 1; r >= 0; --r) {
      asm volatile("s_waitcnt vmcnt(%0)" :: "n"(4 * r) : "memory");
      pub = pub > iss - r ? pub : iss - r;
      ctl[0] = pub;
    }
    STAMP(2);
    return;
  }

  if (wave == NC + 1) {
    u32 tag = __hip_atomic_load((const GAS u32*)(ws + WS_EPOCH), RLX_AGENT);
    if (lane < 16) ctl[lane] = 0u;
    wg_barrier();
    asm volatile("" : "+v"(tag));
    tag = __builtin_amdgcn_readfirstlane(tag) + 1u;
    ctl[3] = tag;
    __builtin_amdgcn_s_sleep(H0_DELAY);
    f32x2 v[2];
    if (SKIP & 16) { v[0] = v[1] = f32x2{1e-3f, 1e-3f}; } else
    sweep2<2, 16>(wsr, lane * 16, WS_H0 + pair * 2048, 1024, tag, v);
    *(LAS f32x2*)(L + L_Q + 8 * lane) = v[0];
    *(LAS f32x2*)(L + L_Q + 512 + 8 * lane) = v[1];
    LGKM0();
    ctl[2] = 1u;
    STAMP(1);
    return;
  }

  const int c = wave - 1, cl = lane & 31;
  wg_barrier();
  const LAS unsigned char* rp = L + L_RING + c * 4096 + lane * 16;
#define TAKE(dst, g, need) do { lds_wait_ge(ctl, (u32)(need)); \
    _Pragma("unroll") for (int i_ = 0; i_ < 4; ++i_) dst[i_] = *(const LAS u32x4*)(rp + ((g) & 3) * 32768 + i_ * 1024); \
    LGKM0(); ctl[8 + c] = (u32)((g) + 1); PIN4(dst); } while (0)
  LAS float* scp = (LAS float*)(L + L_SC + c * 1408);
  LAS float* scw = scp + (lane >> 5) * 8 + ((lane >> 2) & 3) * 2 + ((lane >> 4) & 1);
  const bool last = 8 * (NGRP - 1) + c < nu;
  u32x4 q0, q1;
  float m;
  int g0 = 0;
  {
    u32x4 k0[4], k1[4], k2[4], k3[4], k4[4], k5[4], k6[4], k7[4];
#define PRE(kk, i) if (g0 == (i) && __builtin_amdgcn_readfirstlane(ctl[2]) == 0u) { TAKE(kk, i, c + 8 * (i) + 1); g0 = (i) + 1; }
    PRE(k0, 0) PRE(k1, 1) PRE(k2, 2) PRE(k3, 3) PRE(k4, 4) PRE(k5, 5) PRE(k6, 6) PRE(k7, 7)
    lds_wait_ge(ctl + 2, 1u);
    q0 = *(const LAS u32x4*)(L + L_Q + 16 * cl); q1 = *(const LAS u32x4*)(L + L_Q + 512 + 16 * cl);
    scw[16 * (NGRP - 1)] = -INFINITY;
    m = -INFINITY;
#define POST(kk, i) if (g0 > (i)) { const float s_ = k_unit(kk, q0, q1); scw[16 * (i)] = s_; m = fmaxf(m, s_); }
    POST(k0, 0) POST(k1, 1) POST(k2, 2) POST(k3, 3) POST(k4, 4) POST(k5, 5) POST(k6, 6) POST(k7, 7)
  }
  const int ng = last ? NGRP : NGRP - 1;
#pragma unroll 1
  for (int g = g0; g < ng; ++g) {
    u32x4 k[4];
    TAKE(k, g, 8 * g + c + 1);
    const float s = k_unit(k, q0, q1);
    scw[16 * g] = s;
    m = fmaxf(m, s);
  }
  ctl[8 + c] = (u32)NGRP;
  {
    m = fmaxf(m, dpp<ROR4>(m)); m = fmaxf(m, dpp<ROR8>(m));
    { const f32x2 t = swap32(m, m); m = fmaxf(t[0], t[1]); }
    if ((lane & 47) == 0) ((LAS float*)(L + L_MX))[2 * c + (lane >> 4)] = m;
    LGKM0();
    if (lane == 0) __hip_atomic_fetch_add((LAS u32*)(L + L_CTL + 4), 1u, __ATOMIC_RELAXED, __HIP_MEMORY_SCOPE_WORKGROUP);
    lds_wait_ge(ctl + 1, 8u);
    float M = ((const LAS float*)(L + L_MX))[lane & 15];
    M = fmaxf(M, dpp<XOR2>(M)); M = fmaxf(M, dpp<ROR4>(M)); M = fmaxf(M, dpp<ROR8>(M));
    const float M0 = dpp<0xA0>(M), M1 = dpp<0xF5>(M);
    float l0 = 0.f, l1 = 0.f;
#pragma unroll
    for (int i = 0; i < 2; ++i) {
      if (i == 0 || lane < 24) {
        f32x4 s = *(const LAS f32x4*)(scp + 4 * (lane + 64 * i));
        s[0] = __builtin_amdgcn_exp2f((s[0] - M0) * CEXP); s[1] = __builtin_amdgcn_exp2f((s[1] - M1) * CEXP);
        s[2] = __builtin_amdgcn_exp2f((s[2] - M0) * CEXP); s[3] = __builtin_amdgcn_exp2f((s[3] - M1) * CEXP);
        *(LAS f32x4*)(scp + 4 * (lane + 64 * i)) = s;
        l0 += s[0] + s[2]; l1 += s[1] + s[3];
      }
    }
    l0 = wave_sum(l0); l1 = wave_sum(l1);
    if (lane == 0) *(LAS f32x2*)(L + L_MX + 64 + 8 * c) = f32x2{l0, l1};
    LGKM0();
    u32 old = 0;
    if (lane == 0) old = __hip_atomic_fetch_add((LAS u32*)(L + L_CTL + 16), 1u, __ATOMIC_RELAXED, __HIP_MEMORY_SCOPE_WORKGROUP);
    old = __builtin_amdgcn_readfirstlane(old);
    asm volatile("" ::: "memory");
    if (old == 7u) {
      float Ls = ((const LAS float*)(L + L_MX + 64))[lane & 15];
      Ls += dpp<XOR2>(Ls); Ls += dpp<ROR4>(Ls); Ls += dpp<ROR8>(Ls);
      if (lane < 4) st_gran((GAS u64*)(ws + WS_ML) + pair * 16 + sp * 4 + lane, __builtin_amdgcn_readfirstlane(ctl[3]), __float_as_uint((lane & 2) ? Ls : M));
    }
  }
  f32x2 acc[8];
#pragma unroll
  for (int j = 0; j < 8; ++j) acc[j] = f32x2{0.f, 0.f};
  const LAS float* scr = scp + (lane >> 5) * 8;
  const u32 tag = __builtin_amdgcn_readfirstlane(ctl[3]);
#pragma unroll 1
  for (int g = 0; g < ng; ++g) {
    const f32x4 P0 = *(const LAS f32x4*)(scr + 16 * g), P1 = *(const LAS f32x4*)(scr + 16 * g + 4);
    u32x4 v[4];
    TAKE(v, NGRP + g, nu + 8 * g + c + 1);
    if (c == HINT_C && g == HINT_G) st_gran((GAS u64*)(ws + WS_HINT) + lane * 256 + kvh * 24 + jj, tag, 1u);
    v_piece(acc, P0[0], P0[1], v[0]); v_piece(acc, P0[2], P0[3], v[1]);
    v_piece(acc, P1[0], P1[1], v[2]); v_piece(acc, P1[2], P1[3], v[3]);
  }
  if (c == 1) STAMP(3);
  {
    float o[8];
#pragma unroll
    for (int j = 0; j < 8; ++j) { const f32x2 t = swap32(acc[j >> 1][j & 1], acc[4 + (j >> 1)][j & 1]); o[j] = t[0] + t[1]; }
    LAS float* dst = (LAS float*)(L + L_RING + c * 4096) + (lane >> 5) * 256 + 8 * cl;
    *(LAS f32x4*)dst = f32x4{o[0], o[1], o[2], o[3]};
    *(LAS f32x4*)(dst + 4) = f32x4{o[4], o[5], o[6], o[7]};
  }
  wg_barrier();
  if (c < 4) {
    const LAS f32x2* pp = (const LAS f32x2*)(L + L_RING) + c * 64 + lane;
    const f32x2 o = ((pp[0] + pp[512]) + (pp[1024] + pp[1536])) + ((pp[2048] + pp[2560]) + (pp[3072] + pp[3584]));
    st_gran2(wsr, lane * 16, WS_H1 + (((4 * kvh + c) * 8 + b) * 3 + sp) * 1024, tag, o[0], o[1]);
  }
  if (c == 1) STAMP(4);
}

extern "C" void kernel_launch(void* const* d_in, const int* in_sizes, int n_in, void* d_out, int out_size, void* d_ws,
                              size_t ws_size, hipStream_t stream) {
  (void)hipFuncSetAttribute((const void*)fused_decode, hipFuncAttributeMaxDynamicSharedMemorySize, L_BYTES);
  fused_decode<<<256, NTHR, L_BYTES, stream>>>((const u16*)d_in[0], (const u16*)d_in[1], (const u16*)d_in[2], (const u16*)d_in[3],
                                               (const u16*)d_in[4], (const u16*)d_in[6], (const u16*)d_in[7], (u16*)d_out,
                                               (unsigned char*)d_ws);
}
```

```cpp
#define POLK "nt"
#define POLV "nt"
#include <hip/hip_runtime.h>
#include <hip/hip_bf16.h>
#include <stdint.h>
#include <stdio.h>

typedef unsigned u32;
typedef unsigned long long u64;
typedef unsigned short u16;
typedef unsigned u32x4 __attribute__((ext_vector_type(4)));
typedef unsigned u32x2 __attribute__((ext_vector_type(2)));
typedef float f32x4 __attribute__((ext_vector_type(4)));
typedef float f32x2 __attribute__((ext_vector_type(2)));
typedef short s16x4 __attribute__((ext_vector_type(4)));
typedef short s16x8 __attribute__((ext_vector_type(8)));
typedef __attribute__((ext_vector_type(2))) __bf16 bf16x2;
typedef __attribute__((ext_vector_type(8))) __bf16 bf16x8;
#define GAS __attribute__((address_space(1)))
#define LAS __attribute__((address_space(3)))
#define RLX_AGENT __ATOMIC_RELAXED, __HIP_MEMORY_SCOPE_AGENT

#define WIN 44
#define WAUX 2
#define H0_DELAY 100
#define HINT_G 20
#define HINT_C 0
#define SKIP 0
#define STAMPS 0

constexpr int B = 8, D = 448, HQ = 16, HKV = 8, SK = 4096, DH = 256, DFF = 1792;
constexpr int NC = 8, NTHR = (NC + 2) * 64;
constexpr int NSLOT = 32, NGRP = 22;
constexpr float CEXP = 0.0625f * 1.4426950408889634f;
constexpr unsigned SPIN_CAP = 1u << 18;

constexpr u32 WS_EPOCH = 0;
constexpr u32 WS_H0 = 65536;
constexpr u32 WS_ML = WS_H0 + 64 * 256 * 8;
constexpr u32 WS_HINT = WS_ML + 64 * 16 * 8;
constexpr u32 WS_H1 = WS_HINT + 64 * 256 * 8;
constexpr u32 WS_HF = WS_H1 + 32 * 8 * 3 * 128 * 8;
constexpr u32 WS_H2 = WS_HF + 4 * 28 * 56 * 32 * 8;
constexpr u32 WS_ST = WS_H2 + 4 * 28 * 32 * 32 * 8;

constexpr int L_RING = 0;
constexpr int L_Q = NSLOT * 4096;
constexpr int L_SC = L_Q + 1024;
constexpr int L_MX = L_SC + 8 * 1408;
constexpr int L_CTL = L_MX + 128;
constexpr int XN_STRIDE = 912, HA_STRIDE = 80, OA_STRIDE = 272;
constexpr int S_WQ = 0;
constexpr int S_W1 = S_WQ + 57344;
constexpr int S_W2 = S_W1 + 28672;
constexpr int S_XN = S_W2 + 28672;
constexpr int S_HA = S_XN + 8 * XN_STRIDE;
constexpr int S_OA = S_HA + 8 * HA_STRIDE;
constexpr int S_TAG = S_OA + 8 * OA_STRIDE;
constexpr int L_BYTES = L_CTL + 64 + 128;
static_assert(L_BYTES <= 160 * 1024 && S_TAG + 16 <= L_BYTES, "LDS");

__device__ __forceinline__ float bf2f(u32 b) { return __uint_as_float(b << 16); }
__device__ __forceinline__ u32 f2bf(float f) { __bf16 b = (__bf16)f; return (u32)__builtin_bit_cast(u16, b); }
__device__ __forceinline__ float rbf(float f) { return bf2f(f2bf(f)); }
__device__ __forceinline__ u32 pk2(float lo, float hi) {
  bf16x2 v = {(__bf16)lo, (__bf16)hi};
  return __builtin_bit_cast(u32, v);
}
__device__ __forceinline__ float dot2(u32 a, u32 b, float acc) {
  return __builtin_amdgcn_fdot2_f32_bf16(__builtin_bit_cast(bf16x2, a), __builtin_bit_cast(bf16x2, b), acc, false);
}
template <int CTRL> __device__ __forceinline__ float dpp(float x) {
  return __builtin_bit_cast(float, __builtin_amdgcn_mov_dpp(__builtin_bit_cast(int, x), CTRL, 0xf, 0xf, true));
}
template <int CTRL, int BANK> __device__ __forceinline__ float dppm(float old, float x) {
  return __builtin_bit_cast(float, __builtin_amdgcn_update_dpp(__builtin_bit_cast(int, old), __builtin_bit_cast(int, x), CTRL, 0xf, BANK, false));
}
constexpr int XOR1 = 0xB1, XOR2 = 0x4E, ROR4 = 0x124, ROR8 = 0x128, SHL4 = 0x104, SHR4 = 0x114, BCAST = 0x150;
__device__ __forceinline__ f32x2 swap16(float a, float b) {
  auto s = __builtin_amdgcn_permlane16_swap(__float_as_uint(a), __float_as_uint(b), false, false);
  return f32x2{__uint_as_float(s[0]), __uint_as_float(s[1])};
}
__device__ __forceinline__ f32x2 swap32(float a, float b) {
  auto s = __builtin_amdgcn_permlane32_swap(__float_as_uint(a), __float_as_uint(b), false, false);
  return f32x2{__uint_as_float(s[0]), __uint_as_float(s[1])};
}
__device__ __forceinline__ float xrows_sum(float x) {
  f32x2 s = swap16(x, x); x = s[0] + s[1];
  f32x2 t = swap32(x, x); return t[0] + t[1];
}
__device__ __forceinline__ float wave_sum(float x) {
  x += dpp<XOR1>(x); x += dpp<XOR2>(x); x += dpp<ROR4>(x); x += dpp<ROR8>(x);
  return xrows_sum(x);
}
typedef __amdgpu_buffer_rsrc_t rsrc_t;
__device__ __forceinline__ rsrc_t mk_rsrc(const void* p, u32 bytes) { return __builtin_amdgcn_make_buffer_rsrc((void*)p, 0, (int)bytes, 0x00020000); }
__device__ __forceinline__ u32x4 ldw(rsrc_t r, u32 voff, u32 soff) { return __builtin_amdgcn_raw_buffer_load_b128(r, voff, soff, WAUX); }
#define LGKM0() asm volatile("s_waitcnt lgkmcnt(0)" ::: "memory")
__device__ __forceinline__ void wg_barrier() {
  LGKM0();
  __builtin_amdgcn_s_barrier();
  asm volatile("" ::: "memory");
}
__device__ __forceinline__ void st_gran(GAS u64* g, u32 tag, u32 v) { __hip_atomic_store(g, ((u64)tag << 32) | (u64)v, RLX_AGENT); }
__device__ __forceinline__ u64 ld_gran(const GAS u64* g) { return __hip_atomic_load(g, RLX_AGENT); }
#define PSLEEP 1
__device__ __forceinline__ void lds_wait_ge(volatile LAS u32* p, u32 need) {
  for (unsigned spins = 0; (u32)__builtin_amdgcn_readfirstlane(*p) < need && spins < (SPIN_CAP << 4); ++spins) __builtin_amdgcn_s_sleep(PSLEEP);
  asm volatile("" ::: "memory");
}
#define PIN8(a) asm volatile("" : "+v"(a[0]), "+v"(a[1]), "+v"(a[2]), "+v"(a[3]), "+v"(a[4]), "+v"(a[5]), "+v"(a[6]), "+v"(a[7]))
#define PIN4(a) asm volatile("" : "+v"(a[0]), "+v"(a[1]), "+v"(a[2]), "+v"(a[3]) : : "memory")

#define STAMP(i) do { } while (0)

__device__ __forceinline__ float k_unit(const u32x4 (&k)[4], const u32x4& q0, const u32x4& q1) {
  float e[4];
#pragma unroll
  for (int i = 0; i < 4; ++i) {
    float a = 0.f, b = 0.f;
#pragma unroll
    for (int j = 0; j < 4; ++j) { a = dot2(k[i][j], q0[j], a); b = dot2(k[i][j], q1[j], b); }
    const f32x2 s = swap16(a, b);
    e[i] = s[0] + s[1];
  }
  const float f0 = dppm<ROR8, 0x3>(e[2], e[0]) + dppm<ROR8, 0xc>(e[0], e[2]);
  const float f1 = dppm<ROR8, 0x3>(e[3], e[1]) + dppm<ROR8, 0xc>(e[1], e[3]);
  float g = dppm<SHL4, 0x5>(f1, f0) + dppm<SHR4, 0xa>(f0, f1);
  g += dpp<XOR1>(g); g += dpp<XOR2>(g);
  return rbf(g);
}
__device__ __forceinline__ void v_piece(f32x2 (&acc)[8], float p0, float p1, u32x4 v) {
#pragma unroll
  for (int j = 0; j < 4; ++j) {
    const f32x2 vv = {__uint_as_float(v[j] << 16), __uint_as_float(v[j] & 0xffff0000u)};
    acc[j] = __builtin_elementwise_fma(vv, f32x2{p0, p0}, acc[j]);
    acc[4 + j] = __builtin_elementwise_fma(vv, f32x2{p1, p1}, acc[4 + j]);
  }
}

#define DMA_W(R, VO, SO, M0) asm volatile("s_mov_b32 m0, %0\n\ts_nop 0\n\tbuffer_load_dwordx4 %1, %2, %3 offen nt lds" :: "s"(M0), "v"(VO), "s"(R), "s"(SO) : "m0", "memory")
#define VM0() asm volatile("s_waitcnt vmcnt(0)" ::: "memory")
__device__ __forceinline__ s16x8 tr_frag(const LAS unsigned char* p, int four_rows) {
  const s16x4 b0 = __builtin_amdgcn_ds_read_tr16_b64_v4i16((LAS s16x4*)p);
  const s16x4 b1 = __builtin_amdgcn_ds_read_tr16_b64_v4i16((LAS s16x4*)(p + four_rows));
  return s16x8{b0[0], b0[1], b0[2], b0[3], b1[0], b1[1], b1[2], b1[3]};
}
__device__ __forceinline__ f32x4 mfma16(s16x8 a, s16x8 b, f32x4 c) {
  return __builtin_amdgcn_mfma_f32_16x16x32_bf16(__builtin_bit_cast(bf16x8, a), __builtin_bit_cast(bf16x8, b), c, 0, 0, 0);
}
template <int KS> __device__ __forceinline__ f32x4 gemm8x16(const LAS unsigned char* A, int astride, const LAS unsigned char* Bt, int bstride, int lane) {
  f32x4 acc = {0.f, 0.f, 0.f, 0.f};
  const int g = lane >> 4;
  const LAS unsigned char* ap = A + (lane & 7) * astride + 16 * g;
  const LAS unsigned char* bp = Bt + (8 * g + ((lane & 15) >> 2)) * bstride + 8 * (lane & 3);
#pragma unroll
  for (int ks = 0; ks < KS; ++ks) acc = mfma16(*(const LAS s16x8*)(ap + 64 * ks), tr_frag(bp + ks * 32 * bstride, 4 * bstride), acc);
  return acc;
}
__device__ __forceinline__ u32 pair_of(f32x4 acc, int lane, int& batch) {
  const int g = lane >> 4;
  const float lo = (g >> 1) ? acc[2] : acc[0], hi = (g >> 1) ? acc[3] : acc[1];
  const float send = (lane & 1) ? lo : hi, keep = (lane & 1) ? hi : lo;
  const float got = dpp<XOR1>(send);
  batch = 4 * (g & 1) + (g >> 1) * 2 + (lane & 1);
  return (lane & 1) ? pk2(got, keep) : pk2(keep, got);
}
__device__ __forceinline__ void st_gran2(rsrc_t wsr, u32 voff, u32 soff, u32 tag, float a, float b) {
  __builtin_amdgcn_raw_buffer_store_b128(u32x4{__float_as_uint(a), tag, __float_as_uint(b), tag}, wsr, voff + soff, 0, 16);
}
__device__ __forceinline__ void pub_tile(rsrc_t wsr, u32 soff, u32 pstride, f32x4 acc, u32 tag, int lane) {
  const int g = lane >> 4;
  st_gran2(wsr, (u32)(2 * (g & 1) + (g >> 1)) * pstride + (lane & 15) * 16, soff, tag, (g >> 1) ? acc[2] : acc[0], (g >> 1) ? acc[3] : acc[1]);
}
__device__ __forceinline__ void nap(int units) {
  for (int i = 0; i < units; ++i) __builtin_amdgcn_s_sleep(127);
}
template <int N, int SLEEP> __device__ __forceinline__ void sweep1(rsrc_t wsr, u32 voff, u32 soff, u32 stride, u32 tag, u32 (&v)[N]) {
  unsigned have = 0;
#pragma unroll
  for (int i = 0; i < N; ++i) v[i] = 0u;
  for (unsigned spins = 0;; ++spins) {
    u32x2 t[N];
    asm volatile("" ::: "memory");
#pragma unroll
    for (int i = 0; i < N; ++i) t[i] = __builtin_amdgcn_raw_buffer_load_b64(wsr, voff, ((have >> i) & 1u) ? 0x40000000u : soff + i * stride, 16);
#pragma unroll
    for (int i = 0; i < N; ++i) {
      v[i] = ((have >> i) & 1u) ? v[i] : t[i][0];
      have |= __all(t[i][1] == tag) ? 1u << i : 0u;
    }
    if (have == (1u << N) - 1u || spins > SPIN_CAP) break;
    __builtin_amdgcn_s_sleep(SLEEP);
  }
  asm volatile("" ::: "memory");
}
template <int N, int SLEEP> __device__ __forceinline__ void sweep2(rsrc_t wsr, u32 voff, u32 soff, u32 stride, u32 tag, f32x2 (&v)[N]) {
  unsigned have = 0;
#pragma unroll
  for (int i = 0; i < N; ++i) v[i] = f32x2{0.f, 0.f};
  for (unsigned spins = 0;; ++spins) {
    u32x4 t[N];
    asm volatile("" ::: "memory");
#pragma unroll
    for (int i = 0; i < N; ++i) t[i] = __builtin_amdgcn_raw_buffer_load_b128(wsr, voff, ((have >> i) & 1u) ? 0x40000000u : soff + i * stride, 16);
#pragma unroll
    for (int i = 0; i < N; ++i) {
      v[i] = ((have >> i) & 1u) ? v[i] : f32x2{__uint_as_float(t[i][0]), __uint_as_float(t[i][2])};
      have |= __all(t[i][1] == tag && t[i][3] == tag) ? 1u << i : 0u;
    }
    if (have == (1u << N) - 1u || spins > SPIN_CAP) break;
    __builtin_amdgcn_s_sleep(SLEEP);
  }
  asm volatile("" ::: "memory");
}

__global__ void __launch_bounds__(NTHR, 3)
fused_decode(const u16* __restrict__ x_, const u16* __restrict__ kc_, const u16* __restrict__ vc_, const u16* __restrict__ wq_,
             const u16* __restrict__ wo_, const u16* __restrict__ w1_, const u16* __restrict__ w2_, u16* __restrict__ out_,
             unsigned char* ws_) {
  extern __shared__ __attribute__((aligned(16))) unsigned char smem[];
  LAS unsigned char* L = (LAS unsigned char*)smem;
  GAS unsigned char* ws = (GAS unsigned char*)ws_;
  const int tid = threadIdx.x, lane = tid & 63, wave = __builtin_amdgcn_readfirstlane(tid >> 6);
  const int bid = blockIdx.x, kvh = bid & 7, jj = bid >> 3;
  const u32 ldsb = (u32)(uintptr_t)L;
  const rsrc_t wsr = mk_rsrc(ws_, WS_ST);

  if (jj >= 24) {
    const int t = jj - 24, c = wave - 1;
    const bool mid = wave >= 1 && wave <= 8;
    if (wave == 0) STAMP(0);
    u32 tag;
    if (mid) {
      u32x4 xw;
      asm volatile("global_load_dwordx4 %0, %1, off" : "=v"(xw) : "v"(x_ + c * D + 8 * (lane < 56 ? lane : 55)) : "memory");
      {
        const rsrc_t r = mk_rsrc(wq_, (SKIP & 8) ? 0u : (u32)D * HQ * DH * 2);
        const u32 vo = (lane >> 3) * (HQ * DH * 2) + (lane & 7) * 16;
#pragma unroll
        for (int it = 0; it < 7; ++it) DMA_W(r, vo, (56 * c + 8 * it) * (HQ * DH * 2) + kvh * 1024 + t * 128, ldsb + S_WQ + (7 * c + it) * 1024);
      }
      asm volatile("s_waitcnt vmcnt(7)" : "+v"(xw) :: "memory");
      float xv[8];
#pragma unroll
      for (int j = 0; j < 4; ++j) { xv[2 * j] = __uint_as_float(xw[j] << 16); xv[2 * j + 1] = __uint_as_float(xw[j] & 0xffff0000u); }
      float s = ((xv[0] + xv[1]) + (xv[2] + xv[3])) + ((xv[4] + xv[5]) + (xv[6] + xv[7]));
      const float mean = wave_sum(lane < 56 ? s : 0.f) * (1.f / D);
      float ss = 0.f;
#pragma unroll
      for (int j = 0; j < 8; ++j) { const float d = xv[j] - mean; ss = fmaf(d, d, ss); }
      const float var = rbf(wave_sum(lane < 56 ? ss : 0.f) * (1.f / D));
      const float mb = rbf(mean), sd = rbf(sqrtf(rbf(var + 1e-5f)));
      u32x4 o;
#pragma unroll
      for (int j = 0; j < 4; ++j) o[j] = pk2(rbf(xv[2 * j] - mb) / sd, rbf(xv[2 * j + 1] - mb) / sd);
      if (lane < 56) *(LAS u32x4*)(L + S_XN + c * XN_STRIDE + 16 * lane) = o;
      VM0();
    } else {
      if (wave == 0) {
        const u32 e = __hip_atomic_load((const GAS u32*)(ws + WS_EPOCH), RLX_AGENT);
        if (lane == 0) *(LAS u32*)(L + S_TAG) = e + 1u;
      }
    }
    wg_barrier();
    tag = __builtin_amdgcn_readfirstlane(*(const LAS u32*)(L + S_TAG));
    if (wave >= 1 && wave <= 4) {
      const f32x4 acc = gemm8x16<14>(L + S_XN, XN_STRIDE, L + S_WQ + 32 * c, 128, lane);
      int bb; const u32 v = pair_of(acc, lane, bb);
      st_gran((GAS u64*)(ws + WS_H0) + (size_t)(bb * 8 + kvh) * 256 + 32 * t + 8 * c + ((lane & 15) >> 1), tag, v);
      if (wave == 1) STAMP(9);
    } else if (t < 7) {
      const int w = wave == 0 ? 0 : wave - 4;
      const rsrc_t r1 = mk_rsrc(w1_, (u32)D * DFF * 2), r2 = mk_rsrc(w2_, (u32)D * DFF * 2);
      const u32 vo = (lane >> 2) * (DFF * 2) + (lane & 3) * 16;
      for (int it = w; it < ((SKIP & 1) ? 0 : 28); it += 6) DMA_W(r1, vo, it * 16 * (DFF * 2) + kvh * 448 + t * 64, ldsb + S_W1 + it * 1024);
      for (int it = w; it < ((SKIP & 1) ? 0 : 28); it += 6) DMA_W(r2, (u32)lane * 16, (kvh * 224 + t * 32) * (D * 2) + it * 1024, ldsb + S_W2 + it * 1024);
      VM0();
    }
    wg_barrier();
    if (mid) {
      const rsrc_t r = mk_rsrc(wo_, (u32)D * HQ * DH * 2);
#pragma unroll
      for (int it = 0; it < ((SKIP & 4) ? 0 : 7); ++it) {
        const int i = lane + 64 * (7 * c + it), row = i / 28;
        DMA_W(r, (u32)(row * (D * 2) + (i - row * 28) * 16), (4 * kvh + (t >> 1)) * 128 * (D * 2) + (t & 1) * 448, ldsb + S_WQ + (7 * c + it) * 1024);
      }
    } else if (t < 7) {
      const int half = wave != 0;
      f32x4 acc = gemm8x16<14>(L + S_XN, XN_STRIDE, L + S_W1 + 32 * half, 64, lane);
#pragma unroll
      for (int i = 0; i < 4; ++i) acc[i] = fmaxf(acc[i], 0.f);
      int bb; const u32 v = pair_of(acc, lane, bb);
      *(LAS u32*)(L + S_HA + bb * HA_STRIDE + half * 32 + 4 * ((lane & 15) >> 1)) = v;
    }
    wg_barrier();
    if (t < 7 && wave >= 1 && wave <= 7 && !(SKIP & 2)) {
#pragma unroll
      for (int i = 0; i < 4; ++i) {
        const f32x4 acc = gemm8x16<1>(L + S_HA, HA_STRIDE, L + S_W2 + 32 * (4 * c + i), D * 2, lane);
        pub_tile(wsr, WS_HF + ((4 * c + i) * 56 + kvh * 7 + t) * 256, 28 * 56 * 256, acc, tag, lane);
      }
    }
    VM0();
    wg_barrier();
    if (mid) {
      s16x8 wb[2][4];
      {
        const LAS unsigned char* bp = L + S_WQ + (8 * (lane >> 4) + ((lane & 15) >> 2)) * 448 + 64 * (c < 7 ? c : 6) + 8 * (lane & 3);
#pragma unroll
        for (int i = 0; i < 2; ++i)
#pragma unroll
          for (int ks = 0; ks < 4; ++ks) wb[i][ks] = tr_frag(bp + 32 * i + ks * 32 * 448, 4 * 448);
        asm volatile("" : "+v"(wb[0][0]), "+v"(wb[0][1]), "+v"(wb[0][2]), "+v"(wb[0][3]), "+v"(wb[1][0]), "+v"(wb[1][1]), "+v"(wb[1][2]), "+v"(wb[1][3]));
      }
      float w0, w1, w2;
      {
        nap(5);
        u32 v[1];
        sweep1<1, 100>(wsr, (u32)((lane & 15) < 12 ? (lane & 15) : 0) * 8, WS_ML + (c * 8 + kvh) * 128, 0, tag, v);
        const int h = t >> 2;
        float mv[3], lv[3];
#pragma unroll
        for (int s = 0; s < 3; ++s) {
          mv[s] = __uint_as_float(__builtin_amdgcn_readlane(v[0], 4 * s + h));
          lv[s] = __uint_as_float(__builtin_amdgcn_readlane(v[0], 4 * s + 2 + h));
        }
        const float M = fmaxf(fmaxf(mv[0], mv[1]), mv[2]);
        w0 = __builtin_amdgcn_exp2f((mv[0] - M) * CEXP); w1 = __builtin_amdgcn_exp2f((mv[1] - M) * CEXP); w2 = __builtin_amdgcn_exp2f((mv[2] - M) * CEXP);
        const float Ls = fmaf(w0, lv[0], fmaf(w1, lv[1], w2 * lv[2]));
        w0 /= Ls; w1 /= Ls; w2 /= Ls;
      }
      if (c == 0) STAMP(10);
      {
        u32 hv[1];
        sweep1<1, 16>(wsr, (u32)((lane & 3) < 3 ? (lane & 3) : 0) * 8, WS_HINT + (kvh * 8 + t) * 2048 + (kvh * 24 + c * 3) * 8, 0, tag, hv);
        f32x2 v[3];
        sweep2<3, 1>(wsr, lane * 16, WS_H1 + ((4 * kvh + (t >> 1)) * 8 + c) * 3072, 1024, tag, v);
        *(LAS u32*)(L + S_OA + c * OA_STRIDE + 4 * lane) =
            pk2(fmaf(w0, v[0][0], fmaf(w1, v[1][0], w2 * v[2][0])), fmaf(w0, v[0][1], fmaf(w1, v[1][1], w2 * v[2][1])));
      }
      if (c == 7) STAMP(6);
      wg_barrier();
      if (c < 7) {
        const LAS unsigned char* ap = L + S_OA + (lane & 7) * OA_STRIDE + 16 * (lane >> 4);
        f32x4 s0 = {0.f, 0.f, 0.f, 0.f}, s1 = {0.f, 0.f, 0.f, 0.f};
#pragma unroll
        for (int ks = 0; ks < 4; ++ks) { const s16x8 a = *(const LAS s16x8*)(ap + 64 * ks); s0 = mfma16(a, wb[0][ks], s0); s1 = mfma16(a, wb[1][ks], s1); }
        const u32 so = WS_H2 + ((14 * (t & 1) + 2 * c) * 32 + 4 * kvh + (t >> 1)) * 256;
        pub_tile(wsr, so, 28 * 32 * 256, s0, tag, lane);
        pub_tile(wsr, so + 32 * 256, 28 * 32 * 256, s1, tag, lane);
        if (c == 0) STAMP(21);
        if (STAMPS) { VM0(); if (c == 0) STAMP(22); }
      }
      return;
    }
    if (t == 7) return;
    const int f = 2 * (7 * kvh + t) + (wave != 0), bp = f / 28, ct = f - bp * 28;
    f32x2 gate, xres;
    {
      const GAS u16* xp = (const GAS u16*)x_ + 2 * bp * D + 16 * ct + (lane & 15);
      xres = f32x2{bf2f(xp[0]), bf2f(xp[D])};
      nap(1);
      f32x2 v[14];
      if (SKIP & 2) { for (int i = 0; i < 14; ++i) v[i] = xres; } else
      sweep2<14, 100>(wsr, lane * 16, WS_HF + f * (56 * 256), 1024, tag, v);
      f32x2 s = ((v[0] + v[1]) + (v[2] + v[3])) + ((v[4] + v[5]) + (v[6] + v[7])) + (((v[8] + v[9]) + (v[10] + v[11])) + (v[12] + v[13]));
      const float g0 = rbf(xrows_sum(s[0])), g1 = rbf(xrows_sum(s[1]));
      gate = f32x2{rbf(g0 / (1.f + __expf(-g0))), rbf(g1 / (1.f + __expf(-g1)))};
    }
    if (wave == 0) STAMP(5);
    nap(4);
    { u32 hv[3]; sweep1<3, 32>(wsr, lane * 8, WS_HINT + (kvh * 8 + t) * 2048, 512, tag, hv); }
    wg_barrier();
    if (wave == 0) STAMP(7);
    {
      f32x2 v[8];
      sweep2<8, 1>(wsr, lane * 16, WS_H2 + f * (32 * 256), 1024, tag, v);
      if (wave == 0) STAMP(23);
      const f32x2 s = ((v[0] + v[1]) + (v[2] + v[3])) + ((v[4] + v[5]) + (v[6] + v[7]));
      const f32x2 u = swap16(s[0], s[1]);
      const float h = u[0] + u[1];
      const f32x2 w = swap32(h, h);
      const float sv = rbf(w[0] + w[1]);
      const int odd = (lane >> 4) & 1;
      const float r = (odd ? xres[1] : xres[0]) + rbf(sv * (odd ? gate[1] : gate[0]));
      if (lane < 32) out_[(2 * bp + odd) * D + 16 * ct + (lane & 15)] = (u16)f2bf(r);
      if (f == 0 && lane == 0) __hip_atomic_store((GAS u32*)(ws + WS_EPOCH), tag, RLX_AGENT);
    }
    if (wave == 0) STAMP(8);
    return;
  }

  volatile LAS u32* ctl = (volatile LAS u32*)(L + L_CTL);
  const int b = jj / 3, sp = jj - 3 * b, pair = b * 8 + kvh;
  const int nu = sp == 2 ? 170 : 171;

  if (wave == 0) {
    __builtin_amdgcn_s_setprio(3);
    STAMP(0);
    const rsrc_t kr = mk_rsrc(kc_, (u32)B * HKV * SK * DH * 2), vr = mk_rsrc(vc_, (u32)B * HKV * SK * DH * 2);
    const u32 so0 = (u32)pair * (SK * DH * 2) + (u32)sp * (171 * 4096), vo = lane * 16;
    constexpr int WU = WIN / 4;
#define DMA_UNIT_P(POL, R, SO, M0, PUB) asm volatile("ds_read_b32 %0, %8\n\ts_mov_b32 m0, %1\n\t" \
      "s_waitcnt vmcnt(%5)\n\tbuffer_load_dwordx4 %2, %3, %4 offen " POL " lds\n\t" \
      "s_waitcnt vmcnt(%5)\n\tbuffer_load_dwordx4 %2, %3, %4 offen offset:1024 " POL " lds\n\t" \
      "s_waitcnt vmcnt(%5)\n\tbuffer_load_dwordx4 %2, %3, %4 offen offset:2048 " POL " lds\n\t" \
      "s_waitcnt vmcnt(%5)\n\tbuffer_load_dwordx4 %2, %3, %4 offen offset:3072 " POL " lds\n\t" \
      "s_waitcnt lgkmcnt(0)\n\tds_write_b32 %6, %7" \
      : "=&v"(dn) : "s"(M0), "v"(vo), "s"(R), "s"(SO), "n"(WIN - 1), "v"(pubaddr), "v"(PUB), "v"(dnaddr) : "m0", "memory")
#define DMA_UNIT(R, SO, M0, PUB) DMA_UNIT_P(POLK, R, SO, M0, PUB)
    u32 dn;
    const u32 dnaddr = ldsb + L_CTL + 32 + 4 * (lane & 7);
    {
      const u32 pubaddr = ldsb + L_CTL + 28;
#pragma unroll
      for (int u = 0; u < WU; ++u) DMA_UNIT(kr, so0 + u * 4096, ldsb + u * 4096, 0u);
    }
    wg_barrier();
    const u32 pubaddr = ldsb + L_CTL;
    dn = 0u;
#pragma unroll
    for (int u = WU; u < 16; ++u) DMA_UNIT(kr, so0 + u * 4096, ldsb + u * 4096, (u32)(u - WU + 1));
    u32 pub = 0, iss = 16;
    for (int g = 2; g < 2 * NGRP; ++g) {
      const int gk = g < NGRP ? g : g - NGRP, cnt = gk < NGRP - 1 ? 8 : nu - 8 * (NGRP - 1);
      const int need = g - NSLOT / 8 + 1;
      u32 so = so0 + (u32)gk * 32768, m0 = ldsb + (u32)(g & (NSLOT / 8 - 1)) * 32768;
      const rsrc_t rr = g < NGRP ? kr : vr;
#pragma unroll 1
      for (int i = 0; i < cnt; ++i) {
        if ((int)__builtin_amdgcn_readlane(dn, i) < need) {
          VM0();
          pub = iss;
          ctl[0] = pub;
          lds_wait_ge(ctl + 8 + i, (u32)need);
        }
        const u32 pv = iss - WU + 1;
        pub = pub > pv ? pub : pv;
        if (g < NGRP) DMA_UNIT_P(POLK, rr, so, m0, pub); else DMA_UNIT_P(POLV, rr, so, m0, pub);
        so += 4096; m0 += 4096; ++iss;
      }
    }
#pragma unroll
    for (int r = WU - 1; r >= 0; --r) {
      asm volatile("s_waitcnt vmcnt(%0)" :: "n"(4 * r) : "memory");
      pub = pub > iss - r ? pub : iss - r;
      ctl[0] = pub;
    }
    STAMP(2);
    return;
  }

  if (wave == NC + 1) {
    u32 tag = __hip_atomic_load((const GAS u32*)(ws + WS_EPOCH), RLX_AGENT);
    if (lane < 16) ctl[lane] = 0u;
    wg_barrier();
    asm volatile("" : "+v"(tag));
    tag = __builtin_amdgcn_readfirstlane(tag) + 1u;
    ctl[3] = tag;
    __builtin_amdgcn_s_sleep(H0_DELAY);
    f32x2 v[2];
    if (SKIP & 16) { v[0] = v[1] = f32x2{1e-3f, 1e-3f}; } else
    sweep2<2, 16>(wsr, lane * 16, WS_H0 + pair * 2048, 1024, tag, v);
    *(LAS f32x2*)(L + L_Q + 8 * lane) = v[0];
    *(LAS f32x2*)(L + L_Q + 512 + 8 * lane) = v[1];
    LGKM0();
    ctl[2] = 1u;
    STAMP(1);
    return;
  }

  const int c = wave - 1, cl = lane & 31;
  wg_barrier();
  const LAS unsigned char* rp = L + L_RING + c * 4096 + lane * 16;
#define TAKE(dst, g, need) do { lds_wait_ge(ctl, (u32)(need)); \
    _Pragma("unroll") for (int i_ = 0; i_ < 4; ++i_) dst[i_] = *(const LAS u32x4*)(rp + ((g) & 3) * 32768 + i_ * 1024); \
    LGKM0(); ctl[8 + c] = (u32)((g) + 1); PIN4(dst); } while (0)
  LAS float* scp = (LAS float*)(L + L_SC + c * 1408);
  LAS float* scw = scp + (lane >> 5) * 8 + ((lane >> 2) & 3) * 2 + ((lane >> 4) & 1);
  const bool last = 8 * (NGRP - 1) + c < nu;
  u32x4 q0, q1;
  float m;
  int g0 = 0;
  {
    u32x4 k0[4], k1[4], k2[4], k3[4], k4[4], k5[4], k6[4], k7[4];
#define PRE(kk, i) if (g0 == (i) && __builtin_amdgcn_readfirstlane(ctl[2]) == 0u) { TAKE(kk, i, c + 8 * (i) + 1); g0 = (i) + 1; }
    PRE(k0, 0) PRE(k1, 1) PRE(k2, 2) PRE(k3, 3) PRE(k4, 4) PRE(k5, 5) PRE(k6, 6) PRE(k7, 7)
    lds_wait_ge(ctl + 2, 1u);
    q0 = *(const LAS u32x4*)(L + L_Q + 16 * cl); q1 = *(const LAS u32x4*)(L + L_Q + 512 + 16 * cl);
    scw[16 * (NGRP - 1)] = -INFINITY;
    m = -INFINITY;
#define POST(kk, i) if (g0 > (i)) { const float s_ = k_unit(kk, q0, q1); scw[16 * (i)] = s_; m = fmaxf(m, s_); }
    POST(k0, 0) POST(k1, 1) POST(k2, 2) POST(k3, 3) POST(k4, 4) POST(k5, 5) POST(k6, 6) POST(k7, 7)
  }
  const int ng = last ? NGRP : NGRP - 1;
#pragma unroll 1
  for (int g = g0; g < ng; ++g) {
    u32x4 k[4];
    TAKE(k, g, 8 * g + c + 1);
    const float s = k_unit(k, q0, q1);
    scw[16 * g] = s;
    m = fmaxf(m, s);
  }
  ctl[8 + c] = (u32)NGRP;
  {
    m = fmaxf(m, dpp<ROR4>(m)); m = fmaxf(m, dpp<ROR8>(m));
    { const f32x2 t = swap32(m, m); m = fmaxf(t[0], t[1]); }
    if ((lane & 47) == 0) ((LAS float*)(L + L_MX))[2 * c + (lane >> 4)] = m;
    LGKM0();
    if (lane == 0) __hip_atomic_fetch_add((LAS u32*)(L + L_CTL + 4), 1u, __ATOMIC_RELAXED, __HIP_MEMORY_SCOPE_WORKGROUP);
    lds_wait_ge(ctl + 1, 8u);
    float M = ((const LAS float*)(L + L_MX))[lane & 15];
    M = fmaxf(M, dpp<XOR2>(M)); M = fmaxf(M, dpp<ROR4>(M)); M = fmaxf(M, dpp<ROR8>(M));
    const float M0 = dpp<0xA0>(M), M1 = dpp<0xF5>(M);
    float l0 = 0.f, l1 = 0.f;
#pragma unroll
    for (int i = 0; i < 2; ++i) {
      if (i == 0 || lane < 24) {
        f32x4 s = *(const LAS f32x4*)(scp + 4 * (lane + 64 * i));
        s[0] = __builtin_amdgcn_exp2f((s[0] - M0) * CEXP); s[1] = __builtin_amdgcn_exp2f((s[1] - M1) * CEXP);
        s[2] = __builtin_amdgcn_exp2f((s[2] - M0) * CEXP); s[3] = __builtin_amdgcn_exp2f((s[3] - M1) * CEXP);
        *(LAS f32x4*)(scp + 4 * (lane + 64 * i)) = s;
        l0 += s[0] + s[2]; l1 += s[1] + s[3];
      }
    }
    l0 = wave_sum(l0); l1 = wave_sum(l1);
    if (lane == 0) *(LAS f32x2*)(L + L_MX + 64 + 8 * c) = f32x2{l0, l1};
    LGKM0();
    u32 old = 0;
    if (lane == 0) old = __hip_atomic_fetch_add((LAS u32*)(L + L_CTL + 16), 1u, __ATOMIC_RELAXED, __HIP_MEMORY_SCOPE_WORKGROUP);
    old = __builtin_amdgcn_readfirstlane(old);
    asm volatile("" ::: "memory");
    if (old == 7u) {
      float Ls = ((const LAS float*)(L + L_MX + 64))[lane & 15];
      Ls += dpp<XOR2>(Ls); Ls += dpp<ROR4>(Ls); Ls += dpp<ROR8>(Ls);
      if (lane < 4) st_gran((GAS u64*)(ws + WS_ML) + pair * 16 + sp * 4 + lane, __builtin_amdgcn_readfirstlane(ctl[3]), __float_as_uint((lane & 2) ? Ls : M));
    }
  }
  f32x2 acc[8];
#pragma unroll
  for (int j = 0; j < 8; ++j) acc[j] = f32x2{0.f, 0.f};
  const LAS float* scr = scp + (lane >> 5) * 8;
  const u32 tag = __builtin_amdgcn_readfirstlane(ctl[3]);
#pragma unroll 1
  for (int g = 0; g < ng; ++g) {
    const f32x4 P0 = *(const LAS f32x4*)(scr + 16 * g), P1 = *(const LAS f32x4*)(scr + 16 * g + 4);
    u32x4 v[4];
    TAKE(v, NGRP + g, nu + 8 * g + c + 1);
    if (c == HINT_C && g == HINT_G) st_gran((GAS u64*)(ws + WS_HINT) + lane * 256 + kvh * 24 + jj, tag, 1u);
    v_piece(acc, P0[0], P0[1], v[0]); v_piece(acc, P0[2], P0[3], v[1]);
    v_piece(acc, P1[0], P1[1], v[2]); v_piece(acc, P1[2], P1[3], v[3]);
  }
  if (c == 1) STAMP(3);
  {
    float o[8];
#pragma unroll
    for (int j = 0; j < 8; ++j) { const f32x2 t = swap32(acc[j >> 1][j & 1], acc[4 + (j >> 1)][j & 1]); o[j] = t[0] + t[1]; }
    LAS float* dst = (LAS float*)(L + L_RING + c * 4096) + (lane >> 5) * 256 + 8 * cl;
    *(LAS f32x4*)dst = f32x4{o[0], o[1], o[2], o[3]};
    *(LAS f32x4*)(dst + 4) = f32x4{o[4], o[5], o[6], o[7]};
  }
  wg_barrier();
  if (c < 4) {
    const LAS f32x2* pp = (const LAS f32x2*)(L + L_RING) + c * 64 + lane;
    const f32x2 o = ((pp[0] + pp[512]) + (pp[1024] + pp[1536])) + ((pp[2048] + pp[2560]) + (pp[3072] + pp[3584]));
    st_gran2(wsr, lane * 16, WS_H1 + (((4 * kvh + c) * 8 + b) * 3 + sp) * 1024, tag, o[0], o[1]);
  }
  if (c == 1) STAMP(4);
}

extern "C" void kernel_launch(void* const* d_in, const int* in_sizes, int n_in, void* d_out, int out_size, void* d_ws,
                              size_t ws_size, hipStream_t stream) {
  fprintf(stderr, "PTRS:"); for (int i = 0; i < n_in; ++i) fprintf(stderr, " %p", d_in[i]); fprintf(stderr, " out %p ws %p\n", d_out, d_ws);
  { hipPointerAttribute_t a; if (hipPointerGetAttributes(&a, d_in[1]) == hipSuccess) fprintf(stderr, "attr type %d dev %d managed %d flags %u\n", (int)a.type, a.device, a.isManaged, a.allocationFlags);
    void* base; size_t sz; if (hipMemGetAddressRange((hipDeviceptr_t*)&base, &sz, d_in[1]) == hipSuccess) fprintf(stderr, "K range base %p size %zu\n", base, sz);
    if (hipMemGetAddressRange((hipDeviceptr_t*)&base, &sz, d_ws) == hipSuccess) fprintf(stderr, "ws range base %p size %zu\n", base, sz); }
  (void)hipFuncSetAttribute((const void*)fused_decode, hipFuncAttributeMaxDynamicSharedMemorySize, L_BYTES);
  fused_decode<<<256, NTHR, L_BYTES, stream>>>((const u16*)d_in[0], (const u16*)d_in[1], (const u16*)d_in[2], (const u16*)d_in[3],
                                               (const u16*)d_in[4], (const u16*)d_in[6], (const u16*)d_in[7], (u16*)d_out,
                                               (unsigned char*)d_ws);
}
```

```cpp
#include <hip/hip_runtime.h>
#include <hip/hip_bf16.h>
#include <stdint.h>

typedef unsigned u32;
typedef unsigned long long u64;
typedef unsigned short u16;
typedef unsigned u32x4 __attribute__((ext_vector_type(4)));
typedef unsigned u32x2 __attribute__((ext_vector_type(2)));
typedef float f32x4 __attribute__((ext_vector_type(4)));
typedef float f32x2 __attribute__((ext_vector_type(2)));
typedef short s16x4 __attribute__((ext_vector_type(4)));
typedef short s16x8 __attribute__((ext_vector_type(8)));
typedef __attribute__((ext_vector_type(2))) __bf16 bf16x2;
typedef __attribute__((ext_vector_type(8))) __bf16 bf16x8;
#define GAS __attribute__((address_space(1)))
#define LAS __attribute__((address_space(3)))
#define RLX_AGENT __ATOMIC_RELAXED, __HIP_MEMORY_SCOPE_AGENT

#define WIN 20
#define WAUX 0
#define SKIP_FFN 0
#define SKIP_WO 0
#define SKIP_WQ 0
#define SKIP_H0 0
#define H0_DELAY 64
#define HINT_X 6
#define STAMPS 0

constexpr int B = 8, D = 448, HQ = 16, HKV = 8, SK = 4096, DH = 256, DFF = 1792;
constexpr int NC = 8, NTHR = (NC + 2) * 64;
constexpr int NSLOT = 32, NUNIT = 256;
constexpr float CEXP = 0.0625f * 1.4426950408889634f;
constexpr unsigned SPIN_CAP = 1u << 18;

constexpr size_t WS_EPOCH = 0;
constexpr size_t WS_H0 = 65536;
constexpr size_t WS_HH = WS_H0 + 64 * 256 * 8;
constexpr size_t WS_HF = WS_HH + 8 * 896 * 8;
constexpr size_t WS_ML = WS_HF + 8 * 28 * 8 * 16 * 8;
constexpr size_t WS_H1 = WS_ML + 64 * 16 * 8;
constexpr size_t WS_H2 = WS_H1 + 8 * 2 * 4 * 8 * 4 * 64 * 8;
constexpr size_t WS_HINT = WS_H2 + 8 * 28 * 64 * 16 * 8;
constexpr size_t WS_ST = WS_HINT + 256 * 8;

constexpr int L_RING = 0;
constexpr int L_STG = NSLOT * 4096;
constexpr int XN_STRIDE = 912, HA_STRIDE = 464, OA_STRIDE = 144;
constexpr int L_XN = L_STG + 448 * 32;
constexpr int L_Q = L_XN + 8 * XN_STRIDE;
constexpr int L_SC = L_Q + 1024;
constexpr int L_ML = L_SC + 8192;
constexpr int L_F = L_ML + 128;
constexpr int L_FIN = L_F + 64;
constexpr int L_CTL = L_FIN + 128;
constexpr int L_BYTES = L_CTL + 64;
static_assert(L_BYTES <= 160 * 1024, "LDS");

__device__ __forceinline__ float bf2f(u32 b) { return __uint_as_float(b << 16); }
__device__ __forceinline__ u32 f2bf(float f) { __bf16 b = (__bf16)f; return (u32)__builtin_bit_cast(u16, b); }
__device__ __forceinline__ float rbf(float f) { return bf2f(f2bf(f)); }
__device__ __forceinline__ u32 pk2(float lo, float hi) {
  bf16x2 v = {(__bf16)lo, (__bf16)hi};
  return __builtin_bit_cast(u32, v);
}
__device__ __forceinline__ float dot2(u32 a, u32 b, float acc) {
  return __builtin_amdgcn_fdot2_f32_bf16(__builtin_bit_cast(bf16x2, a), __builtin_bit_cast(bf16x2, b), acc, false);
}
template <int CTRL> __device__ __forceinline__ float dpp(float x) {
  return __builtin_bit_cast(float, __builtin_amdgcn_mov_dpp(__builtin_bit_cast(int, x), CTRL, 0xf, 0xf, true));
}
template <int CTRL, int BANK> __device__ __forceinline__ float dppm(float old, float x) {
  return __builtin_bit_cast(float, __builtin_amdgcn_update_dpp(__builtin_bit_cast(int, old), __builtin_bit_cast(int, x), CTRL, 0xf, BANK, false));
}
constexpr int XOR1 = 0xB1, XOR2 = 0x4E, ROR4 = 0x124, ROR8 = 0x128, SHL4 = 0x104, SHR4 = 0x114, BCAST = 0x150;
__device__ __forceinline__ f32x2 swap16(float a, float b) {
  auto s = __builtin_amdgcn_permlane16_swap(__float_as_uint(a), __float_as_uint(b), false, false);
  return f32x2{__uint_as_float(s[0]), __uint_as_float(s[1])};
}
__device__ __forceinline__ f32x2 swap32(float a, float b) {
  auto s = __builtin_amdgcn_permlane32_swap(__float_as_uint(a), __float_as_uint(b), false, false);
  return f32x2{__uint_as_float(s[0]), __uint_as_float(s[1])};
}
__device__ __forceinline__ float xrows_sum(float x) {
  f32x2 s = swap16(x, x); x = s[0] + s[1];
  f32x2 t = swap32(x, x); return t[0] + t[1];
}
__device__ __forceinline__ float wave_sum(float x) {
  x += dpp<XOR1>(x); x += dpp<XOR2>(x); x += dpp<ROR4>(x); x += dpp<ROR8>(x);
  return xrows_sum(x);
}
typedef __amdgpu_buffer_rsrc_t rsrc_t;
__device__ __forceinline__ rsrc_t mk_rsrc(const void* p, u32 bytes) { return __builtin_amdgcn_make_buffer_rsrc((void*)p, 0, (int)bytes, 0x00020000); }
__device__ __forceinline__ u32x4 ldw(rsrc_t r, u32 voff, u32 soff) { return __builtin_amdgcn_raw_buffer_load_b128(r, voff, soff, WAUX); }
#define LGKM0() asm volatile("s_waitcnt lgkmcnt(0)" ::: "memory")
__device__ __forceinline__ void wg_barrier() {
  LGKM0();
  __builtin_amdgcn_s_barrier();
  asm volatile("" ::: "memory");
}
__device__ __forceinline__ void st_gran(GAS u64* g, u32 tag, u32 v) { __hip_atomic_store(g, ((u64)tag << 32) | (u64)v, RLX_AGENT); }
__device__ __forceinline__ u64 ld_gran(const GAS u64* g) { return __hip_atomic_load(g, RLX_AGENT); }
#define PSLEEP 1
__device__ __forceinline__ void lds_wait_ge(volatile LAS u32* p, u32 need) {
  for (unsigned spins = 0; (u32)__builtin_amdgcn_readfirstlane(*p) < need && spins < (SPIN_CAP << 4); ++spins) __builtin_amdgcn_s_sleep(PSLEEP);
  asm volatile("" ::: "memory");
}
#define PIN8(a) asm volatile("" : "+v"(a[0]), "+v"(a[1]), "+v"(a[2]), "+v"(a[3]), "+v"(a[4]), "+v"(a[5]), "+v"(a[6]), "+v"(a[7]))
#define PIN4(a) asm volatile("" : "+v"(a[0]), "+v"(a[1]), "+v"(a[2]), "+v"(a[3]) : : "memory")

#define STAMP(i) do { } while (0)

__device__ __forceinline__ float k_unit(const u32x4 (&k)[4], const u32x4& q0, const u32x4& q1) {
  float e[4];
#pragma unroll
  for (int i = 0; i < 4; ++i) {
    float a = 0.f, b = 0.f;
#pragma unroll
    for (int j = 0; j < 4; ++j) { a = dot2(k[i][j], q0[j], a); b = dot2(k[i][j], q1[j], b); }
    const f32x2 s = swap16(a, b);
    e[i] = s[0] + s[1];
  }
  const float f0 = dppm<ROR8, 0x3>(e[2], e[0]) + dppm<ROR8, 0xc>(e[0], e[2]);
  const float f1 = dppm<ROR8, 0x3>(e[3], e[1]) + dppm<ROR8, 0xc>(e[1], e[3]);
  float g = dppm<SHL4, 0x5>(f1, f0) + dppm<SHR4, 0xa>(f0, f1);
  g += dpp<XOR1>(g); g += dpp<XOR2>(g);
  return rbf(g);
}
__device__ __forceinline__ void v_piece(f32x2 (&acc)[8], float p0, float p1, u32x4 v) {
#pragma unroll
  for (int j = 0; j < 4; ++j) {
    const f32x2 vv = {__uint_as_float(v[j] << 16), __uint_as_float(v[j] & 0xffff0000u)};
    acc[j] = __builtin_elementwise_fma(vv, f32x2{p0, p0}, acc[j]);
    acc[4 + j] = __builtin_elementwise_fma(vv, f32x2{p1, p1}, acc[4 + j]);
  }
}

#define DMA_W(R, VO, SO, M0) asm volatile("s_mov_b32 m0, %0\n\ts_nop 0\n\tbuffer_load_dwordx4 %1, %2, %3 offen lds" :: "s"(M0), "v"(VO), "s"(R), "s"(SO) : "m0", "memory")
#define VM0() asm volatile("s_waitcnt vmcnt(0)" ::: "memory")
__device__ __forceinline__ s16x8 tr_frag(const LAS unsigned char* p, int four_rows) {
  const s16x4 b0 = __builtin_amdgcn_ds_read_tr16_b64_v4i16((LAS s16x4*)p);
  const s16x4 b1 = __builtin_amdgcn_ds_read_tr16_b64_v4i16((LAS s16x4*)(p + four_rows));
  return s16x8{b0[0], b0[1], b0[2], b0[3], b1[0], b1[1], b1[2], b1[3]};
}
__device__ __forceinline__ f32x4 mfma16(s16x8 a, s16x8 b, f32x4 c) {
  return __builtin_amdgcn_mfma_f32_16x16x32_bf16(__builtin_bit_cast(bf16x8, a), __builtin_bit_cast(bf16x8, b), c, 0, 0, 0);
}
template <int KS> __device__ __forceinline__ f32x4 gemm8x16(const LAS unsigned char* A, int astride, const LAS unsigned char* Bt, int lane) {
  f32x4 acc = {0.f, 0.f, 0.f, 0.f};
  const int g = lane >> 4;
  const LAS unsigned char* ap = A + (lane & 7) * astride + 16 * g;
  const LAS unsigned char* bp = Bt + (8 * g + ((lane & 15) >> 2)) * 32 + 8 * (lane & 3);
#pragma unroll
  for (int ks = 0; ks < KS; ++ks) acc = mfma16(*(const LAS s16x8*)(ap + 64 * ks), tr_frag(bp + ks * 1024, 128), acc);
  return acc;
}
__device__ __forceinline__ u32 pair_of(f32x4 acc, int lane, int& batch) {
  const int g = lane >> 4;
  const float lo = (g >> 1) ? acc[2] : acc[0], hi = (g >> 1) ? acc[3] : acc[1];
  const float send = (lane & 1) ? lo : hi, keep = (lane & 1) ? hi : lo;
  const float got = dpp<XOR1>(send);
  batch = 4 * (g & 1) + (g >> 1) * 2 + (lane & 1);
  return (lane & 1) ? pk2(got, keep) : pk2(keep, got);
}
__device__ __forceinline__ void pub_tile(GAS u64* base, int bstride, f32x4 acc, u32 tag, int lane) {
  const int g = lane >> 4, b0 = 4 * (g & 1) + (g >> 1) * 2;
  GAS u64* p = base + (size_t)b0 * bstride + (lane & 15);
  st_gran(p, tag, __float_as_uint((g >> 1) ? acc[2] : acc[0]));
  st_gran(p + bstride, tag, __float_as_uint((g >> 1) ? acc[3] : acc[1]));
}
__device__ __forceinline__ void nap(int units) {
  for (int i = 0; i < units; ++i) __builtin_amdgcn_s_sleep(127);
}
template <int N, int SLEEP> __device__ __forceinline__ void sweep_missing(rsrc_t wsr, u32 voff, u32 soff, u32 stride, u32 tag, u32 (&v)[N]) {
  unsigned have = 0;
#pragma unroll
  for (int i = 0; i < N; ++i) v[i] = 0u;
  for (unsigned spins = 0;; ++spins) {
    u32x2 t[N];
    asm volatile("" ::: "memory");
#pragma unroll
    for (int i = 0; i < N; ++i) t[i] = __builtin_amdgcn_raw_buffer_load_b64(wsr, voff, ((have >> i) & 1u) ? 0x40000000u : soff + i * stride, 16);
#pragma unroll
    for (int i = 0; i < N; ++i) {
      v[i] = ((have >> i) & 1u) ? v[i] : t[i][0];
      have |= __all(t[i][1] == tag) ? 1u << i : 0u;
    }
    if (have == (1u << N) - 1u || spins > SPIN_CAP) break;
    __builtin_amdgcn_s_sleep(SLEEP);
  }
  asm volatile("" ::: "memory");
}
template <int N, int SLEEP> __device__ __forceinline__ void sweep(const GAS u64* g, int stride, u32 tag, u32 (&v)[N]) {
  for (unsigned spins = 0;; ++spins) {
    bool ok = true;
#pragma unroll
    for (int i = 0; i < N; ++i) { const u64 t = ld_gran(g + i * stride); v[i] = (u32)t; ok &= (u32)(t >> 32) == tag; }
    if (__all(ok) || spins > SPIN_CAP) break;
    __builtin_amdgcn_s_sleep(SLEEP);
  }
}

__global__ void __launch_bounds__(NTHR, 3)
fused_decode(const u16* __restrict__ x_, const u16* __restrict__ kc_, const u16* __restrict__ vc_, const u16* __restrict__ wq_,
             const u16* __restrict__ wo_, const u16* __restrict__ w1_, const u16* __restrict__ w2_, u16* __restrict__ out_,
             unsigned char* ws_) {
  extern __shared__ __attribute__((aligned(16))) unsigned char smem[];
  LAS unsigned char* L = (LAS unsigned char*)smem;
  volatile LAS u32* ctl = (volatile LAS u32*)(L + L_CTL);
  GAS unsigned char* ws = (GAS unsigned char*)ws_;
  const int tid = threadIdx.x, lane = tid & 63, wave = __builtin_amdgcn_readfirstlane(tid >> 6);
  const int bid = blockIdx.x, kvh = bid & 7, jj = bid >> 3;
  const int sp = jj & 3, b = jj >> 2, pair = b * 8 + kvh;
  const int hh = jj >> 4, dq = (jj >> 2) & 3, cq = jj & 3;
  const u32 ldsb = (u32)(uintptr_t)L;
  const rsrc_t wsr = mk_rsrc(ws_, (u32)WS_ST);

  if (wave == NC + 1) {
    __builtin_amdgcn_s_setprio(3);
    const rsrc_t kr = mk_rsrc(kc_, (u32)B * HKV * SK * DH * 2), vr = mk_rsrc(vc_, (u32)B * HKV * SK * DH * 2);
    const u32 so0 = (u32)pair * (SK * DH * 2) + (u32)sp * (1024 * DH * 2), vo = lane * 16;
    constexpr int WU = WIN / 4;
#define DMA_UNIT(R, SO, M0, PUB) asm volatile("s_mov_b32 m0, %0\n\t" \
      "s_waitcnt vmcnt(%4)\n\tbuffer_load_dwordx4 %1, %2, %3 offen nt lds\n\t" \
      "s_waitcnt vmcnt(%4)\n\tbuffer_load_dwordx4 %1, %2, %3 offen offset:1024 nt lds\n\t" \
      "s_waitcnt vmcnt(%4)\n\tbuffer_load_dwordx4 %1, %2, %3 offen offset:2048 nt lds\n\t" \
      "s_waitcnt vmcnt(%4)\n\tbuffer_load_dwordx4 %1, %2, %3 offen offset:3072 nt lds\n\t" \
      "ds_write_b32 %5, %6" \
      :: "s"(M0), "v"(vo), "s"(R), "s"(SO), "n"(WIN - 1), "v"(pubaddr), "v"(PUB) : "m0", "memory")
    const u32 pubaddr = ldsb + L_CTL, dummy = ldsb + L_CTL + 28;
    {
      const u32 pubaddr = dummy;
#pragma unroll
      for (int u = 0; u < WU; ++u) DMA_UNIT(kr, so0 + u * 4096, ldsb + u * 4096, 0u);
    }
    wg_barrier();
#pragma unroll
    for (int u = WU; u < 16; ++u) DMA_UNIT(kr, so0 + u * 4096, ldsb + u * 4096, (u32)(u - WU + 1));
    u32 dn = 0, pub = 0;
    for (int n = 2; n < NUNIT / 8; ++n) {
      if (n >= NSLOT / 8) {
        const u32 need = (u32)(n - NSLOT / 8 + 1);
        if (!__all(dn >= need)) {
          asm volatile("s_waitcnt vmcnt(0)" ::: "memory");
          pub = (u32)(8 * n);
          ctl[0] = pub;
          for (unsigned spins = 0; !__all(ctl[8 + (lane & 7)] >= need) && spins < (SPIN_CAP << 4); ++spins) __builtin_amdgcn_s_sleep(1);
          asm volatile("" ::: "memory");
        }
      }
      const u32 so = so0 + (u32)(n & 15) * 32768, m0 = ldsb + (u32)(n & (NSLOT / 8 - 1)) * 32768;
      const rsrc_t rr = n < 16 ? kr : vr;
#pragma unroll
      for (int i = 0; i < 8; ++i) {
        const u32 pv = (u32)(8 * n + i - WU + 1);
        pub = pub > pv ? pub : pv;
        DMA_UNIT(rr, so + i * 4096, m0 + i * 4096, pub);
        if (i == 6) dn = ctl[8 + (lane & 7)];
        if (i == (NUNIT - HINT_X) % 8 && n == (NUNIT - HINT_X) / 8) {
          const u32 tg = __builtin_amdgcn_readfirstlane(ctl[3]);
          if (lane == 0) st_gran((GAS u64*)(ws + WS_HINT) + kvh * 32 + jj, tg, 1u);
        }
      }
    }
#pragma unroll
    for (int r = WU - 1; r >= 0; --r) {
      asm volatile("s_waitcnt vmcnt(%0)" :: "n"(4 * r) : "memory");
      pub = pub > (u32)(NUNIT - r) ? pub : (u32)(NUNIT - r);
      ctl[0] = pub;
    }
    STAMP(2);
    return;
  }

  if (wave == 0) {
    STAMP(0);
    {
      const rsrc_t r = mk_rsrc(wq_, (u32)D * HQ * DH * 2);
      const u32 vo = (lane >> 1) * (HQ * DH * 2) + (lane & 1) * 16;
#pragma unroll
      for (int it = 0; it < (SKIP_WQ ? 0 : 14); ++it) DMA_W(r, vo, it * 32 * (HQ * DH * 2) + kvh * 1024 + jj * 32, ldsb + L_STG + it * 1024);
    }
    u32 tag = __hip_atomic_load((const GAS u32*)(ws + WS_EPOCH), RLX_AGENT);
    if (lane < 16) ctl[lane] = 0u;
    wg_barrier();
    asm volatile("" : "+v"(tag));
    tag = __builtin_amdgcn_readfirstlane(tag) + 1u;
    VM0();
    ctl[3] = tag;
    lds_wait_ge(ctl + 1, 8u);
    {
      const f32x4 acc = gemm8x16<14>(L + L_XN, XN_STRIDE, L + L_STG, lane);
      int bb; const u32 v = pair_of(acc, lane, bb);
      st_gran((GAS u64*)(ws + WS_H0) + (size_t)(bb * 8 + kvh) * 256 + 8 * jj + ((lane & 15) >> 1), tag, v);
    }
    STAMP(9);
    {
      u32 v[4];
      if (SKIP_H0) { v[0] = v[1] = v[2] = v[3] = 0x3c003c00u; } else
      { __builtin_amdgcn_s_sleep(H0_DELAY); sweep_missing<4, 16>(wsr, lane * 8, (u32)WS_H0 + pair * 2048, 512, tag, v); }
#pragma unroll
      for (int i = 0; i < 4; ++i) *(LAS u32*)(L + L_Q + 4 * (lane + 64 * i)) = v[i];
      LGKM0();
      ctl[2] = 1u;
    }
    STAMP(1);
    if (jj < 14 && !SKIP_FFN) {
      const rsrc_t r = mk_rsrc(w1_, (u32)D * DFF * 2);
      const u32 vo = (lane >> 1) * (DFF * 2) + (lane & 1) * 16;
#pragma unroll
      for (int it = 0; it < 14; ++it) DMA_W(r, vo, it * 32 * (DFF * 2) + kvh * 448 + jj * 32, ldsb + L_STG + it * 1024);
      VM0();
      f32x4 acc = gemm8x16<14>(L + L_XN, XN_STRIDE, L + L_STG, lane);
#pragma unroll
      for (int i = 0; i < 4; ++i) acc[i] = fmaxf(acc[i], 0.f);
      int bb; const u32 v = pair_of(acc, lane, bb);
      st_gran((GAS u64*)(ws + WS_HH) + (size_t)(kvh * 8 + bb) * 112 + 8 * jj + ((lane & 15) >> 1), tag, v);
    }
    if (jj < 28 && !SKIP_FFN) {
      const rsrc_t r = mk_rsrc(w2_, (u32)D * DFF * 2);
      const u32 vo = (lane >> 1) * (D * 2) + (lane & 1) * 16;
      LGKM0();
#pragma unroll
      for (int it = 0; it < 7; ++it) DMA_W(r, vo, (kvh * 224 + it * 32) * (D * 2) + jj * 32, ldsb + L_STG + it * 1024);
      u32 v[14];
      nap(jj < 14 ? 1 : 2);
      sweep<14, 64>((const GAS u64*)(ws + WS_HH) + (size_t)kvh * 896 + lane, 64, tag, v);
#pragma unroll
      for (int i = 0; i < 14; ++i) { const int idx = lane + 64 * i, bb = idx / 112; *(LAS u32*)(L + L_XN + bb * HA_STRIDE + 4 * (idx - bb * 112)) = v[i]; }
      VM0(); LGKM0();
      const f32x4 acc = gemm8x16<7>(L + L_XN, HA_STRIDE, L + L_STG, lane);
      pub_tile((GAS u64*)(ws + WS_HF) + (size_t)(jj * 8 + kvh) * 16, 28 * 8 * 16, acc, tag, lane);
    }
    {
      const rsrc_t r = mk_rsrc(wo_, (u32)D * HQ * DH * 2);
      LGKM0();
#pragma unroll
      for (int it = 0; it < (SKIP_WO ? 0 : 14); ++it) {
        const int i = lane + 64 * it, row = i / 14;
        DMA_W(r, (u32)(row * (D * 2) + (i - row * 14) * 16), ((2 * kvh + hh) * 256 + 64 * dq) * (D * 2) + cq * 224, ldsb + L_STG + it * 1024);
      }
      VM0();
      ctl[5] = 1u;
    }
    if (jj < 28 && !SKIP_FFN) {
      const float xres = bf2f(((const GAS u16*)x_)[kvh * D + 16 * jj + (lane & 15)]);
      u32 v[2];
      nap(1);
      sweep<2, 64>((const GAS u64*)(ws + WS_HF) + (size_t)(kvh * 28 + jj) * 128 + lane, 64, tag, v);
      const float t = rbf(xrows_sum(__uint_as_float(v[0]) + __uint_as_float(v[1])));
      const float gate = rbf(t / (1.f + __expf(-t)));
      if (lane < 16) { ((LAS float*)(L + L_FIN))[lane] = gate; ((LAS float*)(L + L_FIN))[16 + lane] = xres; }
    }
    STAMP(5);
    {
      lds_wait_ge(ctl + 4, 8u);
      const int w = (lane >> 1) & 7, h = lane & 1;
      const f32x2 ml = *(const LAS f32x2*)(L + L_ML + w * 16 + h * 8);
      float M = ml[0];
      M = fmaxf(M, dpp<XOR2>(M)); M = fmaxf(M, dpp<ROR4>(M)); M = fmaxf(M, dpp<ROR8>(M));
      float Ls = ml[1] * __builtin_amdgcn_exp2f((ml[0] - M) * CEXP);
      Ls += dpp<XOR2>(Ls); Ls += dpp<ROR4>(Ls); Ls += dpp<ROR8>(Ls);
      GAS u64* g = (GAS u64*)(ws + WS_ML) + pair * 16;
      if (lane < 4) st_gran(g + sp * 4 + lane, tag, __float_as_uint((lane & 2) ? Ls : M));
      u32 v[2];
      __builtin_amdgcn_s_sleep(64);
      sweep<2, 40>(g + (lane >> 4) * 4 + h, 2, tag, v);
      const float Ms = __uint_as_float(v[0]);
      float Mg = Ms;
      { const f32x2 t = swap16(Mg, Mg); Mg = fmaxf(t[0], t[1]); }
      { const f32x2 t = swap32(Mg, Mg); Mg = fmaxf(t[0], t[1]); }
      const float Lg = xrows_sum(__uint_as_float(v[1]) * __builtin_amdgcn_exp2f((Ms - Mg) * CEXP));
      if (lane < 16) ((LAS float*)(L + L_F))[lane] = __builtin_amdgcn_exp2f((ml[0] - Mg) * CEXP) / Lg;
      LGKM0();
      ctl[6] = 1u;
    }
    STAMP(10);
    return;
  }

  const int c = wave - 1, cl = lane & 31;
  {
    const u32x4 xw = *(const GAS u32x4*)((const GAS u16*)x_ + c * D + 8 * (lane < 56 ? lane : 55));
    wg_barrier();
    float xv[8];
#pragma unroll
    for (int j = 0; j < 4; ++j) { xv[2 * j] = __uint_as_float(xw[j] << 16); xv[2 * j + 1] = __uint_as_float(xw[j] & 0xffff0000u); }
    float s = ((xv[0] + xv[1]) + (xv[2] + xv[3])) + ((xv[4] + xv[5]) + (xv[6] + xv[7]));
    const float mean = wave_sum(lane < 56 ? s : 0.f) * (1.f / D);
    float ss = 0.f;
#pragma unroll
    for (int j = 0; j < 8; ++j) { const float d = xv[j] - mean; ss = fmaf(d, d, ss); }
    const float var = rbf(wave_sum(lane < 56 ? ss : 0.f) * (1.f / D));
    const float mb = rbf(mean), sd = rbf(sqrtf(rbf(var + 1e-5f)));
    u32x4 o;
#pragma unroll
    for (int j = 0; j < 4; ++j) o[j] = pk2(rbf(xv[2 * j] - mb) / sd, rbf(xv[2 * j + 1] - mb) / sd);
    if (lane < 56) *(LAS u32x4*)(L + L_XN + c * XN_STRIDE + 16 * lane) = o;
    LGKM0();
    if (lane == 0) __hip_atomic_fetch_add((LAS u32*)(L + L_CTL + 4), 1u, __ATOMIC_RELAXED, __HIP_MEMORY_SCOPE_WORKGROUP);
  }
  const LAS unsigned char* rp = L + L_RING + c * 4096 + lane * 16;
#define TAKE(dst, n) do { lds_wait_ge(ctl, (u32)(8 * (n) + c + 1)); \
    _Pragma("unroll") for (int i_ = 0; i_ < 4; ++i_) dst[i_] = *(const LAS u32x4*)(rp + ((n) & 3) * 32768 + i_ * 1024); \
    LGKM0(); ctl[8 + c] = (u32)((n) + 1); PIN4(dst); } while (0)
  LAS float* scp = (LAS float*)(L + L_SC + c * 1024);
  LAS float* scw = scp + (lane >> 5) * 8 + ((lane >> 2) & 3) * 2 + ((lane >> 4) & 1);
  u32x4 q0, q1;
  float m;
  {
    u32x4 k0[4], k1[4], k2[4], k3[4];
    TAKE(k0, 0); TAKE(k1, 1); TAKE(k2, 2); TAKE(k3, 3);
    lds_wait_ge(ctl + 2, 1u);
    q0 = *(const LAS u32x4*)(L + L_Q + 16 * cl); q1 = *(const LAS u32x4*)(L + L_Q + 512 + 16 * cl);
    const float s0 = k_unit(k0, q0, q1), s1 = k_unit(k1, q0, q1), s2 = k_unit(k2, q0, q1), s3 = k_unit(k3, q0, q1);
    scw[0] = s0; scw[16] = s1; scw[32] = s2; scw[48] = s3;
    m = fmaxf(fmaxf(s0, s1), fmaxf(s2, s3));
  }
#pragma unroll 1
  for (int n = 4; n < 16; ++n) {
    u32x4 k[4];
    TAKE(k, n);
    const float s = k_unit(k, q0, q1);
    scw[16 * n] = s;
    m = fmaxf(m, s);
  }
  {
    m = fmaxf(m, dpp<ROR4>(m)); m = fmaxf(m, dpp<ROR8>(m));
    { const f32x2 t = swap32(m, m); m = fmaxf(t[0], t[1]); }
    const f32x2 mm = swap16(m, m);
    LGKM0();
    f32x4 s = *(const LAS f32x4*)(scp + 4 * lane);
    s[0] = __builtin_amdgcn_exp2f((s[0] - mm[0]) * CEXP); s[1] = __builtin_amdgcn_exp2f((s[1] - mm[1]) * CEXP);
    s[2] = __builtin_amdgcn_exp2f((s[2] - mm[0]) * CEXP); s[3] = __builtin_amdgcn_exp2f((s[3] - mm[1]) * CEXP);
    *(LAS f32x4*)(scp + 4 * lane) = s;
    const float l0 = wave_sum(s[0] + s[2]), l1 = wave_sum(s[1] + s[3]);
    if (lane == 0) *(LAS f32x4*)(L + L_ML + c * 16) = f32x4{mm[0], l0, mm[1], l1};
    LGKM0();
    if (lane == 0) __hip_atomic_fetch_add((LAS u32*)(L + L_CTL + 16), 1u, __ATOMIC_RELAXED, __HIP_MEMORY_SCOPE_WORKGROUP);
  }
  f32x2 acc[8];
#pragma unroll
  for (int j = 0; j < 8; ++j) acc[j] = f32x2{0.f, 0.f};
  const LAS float* scr = scp + (lane >> 5) * 8;
  auto v_step = [&](int n) {
    const f32x4 P0 = *(const LAS f32x4*)(scr + 16 * (n - 16)), P1 = *(const LAS f32x4*)(scr + 16 * (n - 16) + 4);
    u32x4 v[4];
    TAKE(v, n);
    v_piece(acc, P0[0], P0[1], v[0]); v_piece(acc, P0[2], P0[3], v[1]);
    v_piece(acc, P1[0], P1[1], v[2]); v_piece(acc, P1[2], P1[3], v[3]);
  };
#pragma unroll 1
  for (int n = 16; n < 31; ++n) v_step(n);
  lds_wait_ge(ctl + 5, 1u);
  lds_wait_ge(ctl + 6, 1u);
  const u32 tag = __builtin_amdgcn_readfirstlane(ctl[3]);
  const f32x2 fs = *(const LAS f32x2*)(L + L_F + c * 8);
  s16x8 wb0, wb1;
  {
    const LAS unsigned char* bp = L + L_STG + (8 * (lane >> 4) + ((lane & 15) >> 2)) * 224 + 32 * (c < 7 ? c : 6) + 8 * (lane & 3);
    wb0 = tr_frag(bp, 4 * 224); wb1 = tr_frag(bp + 32 * 224, 4 * 224);
    asm volatile("" : "+v"(wb0), "+v"(wb1));
  }
  v_step(31);
  if (c == 7) STAMP(3);
  {
    float o[8];
#pragma unroll
    for (int j = 0; j < 8; ++j) { const f32x2 t = swap32(acc[j >> 1][j & 1] * fs[0], acc[4 + (j >> 1)][j & 1] * fs[1]); o[j] = t[0] + t[1]; }
    LAS float* dst = (LAS float*)(L + L_RING + c * 4096) + (lane >> 5) * 256 + 8 * cl;
    *(LAS f32x4*)dst = f32x4{o[0], o[1], o[2], o[3]};
    *(LAS f32x4*)(dst + 4) = f32x4{o[4], o[5], o[6], o[7]};
  }
  wg_barrier();
  {
    const LAS float* pp = (const LAS float*)(L + L_RING) + c * 64 + lane;
    const float o = ((pp[0] + pp[1024]) + (pp[2048] + pp[3072])) + ((pp[4096] + pp[5120]) + (pp[6144] + pp[7168]));
    st_gran((GAS u64*)(ws + WS_H1) + ((size_t)((kvh * 8 + c) * 8 + b) * 4 + sp) * 64 + lane, tag, __float_as_uint(o));
  }
  if (c == 7) STAMP(4);
  {
    u32 v[4];
    { u32 hv[1]; sweep<1, 8>((const GAS u64*)(ws + WS_HINT) + kvh * 32 + c * 4 + (lane & 3), 0, tag, hv); }
    sweep_missing<4, 1>(wsr, lane * 8, (u32)WS_H1 + ((kvh * 8 + hh * 4 + dq) * 8 + c) * 2048, 512, tag, v);
    const float o = (__uint_as_float(v[0]) + __uint_as_float(v[1])) + (__uint_as_float(v[2]) + __uint_as_float(v[3]));
    *(LAS u16*)(L + L_XN + c * OA_STRIDE + 2 * lane) = (u16)f2bf(o);
  }
  if (c == 7) STAMP(6);
  wg_barrier();
  if (c < 7) {
    const LAS unsigned char* ap = L + L_XN + (lane & 7) * OA_STRIDE + 16 * (lane >> 4);
    f32x4 s = {0.f, 0.f, 0.f, 0.f};
    s = mfma16(*(const LAS s16x8*)ap, wb0, s);
    s = mfma16(*(const LAS s16x8*)(ap + 64), wb1, s);
    pub_tile((GAS u64*)(ws + WS_H2) + (size_t)((7 * cq + c) * 64 + (kvh * 8 + hh * 4 + dq)) * 16, 28 * 64 * 16, s, tag, lane);
    return;
  }
  STAMP(7);
  if (jj < 28) {
    const float gate = ((const LAS float*)(L + L_FIN))[lane & 15], xres = ((const LAS float*)(L + L_FIN))[16 + (lane & 15)];
    u32 v[16];
    { u32 hv[4]; sweep_missing<4, 8>(wsr, lane * 8, (u32)WS_HINT, 512, tag, hv); }
    sweep_missing<16, 1>(wsr, lane * 8, (u32)WS_H2 + (kvh * 28 + jj) * 8192, 512, tag, v);
    float f[8];
#pragma unroll
    for (int i = 0; i < 8; ++i) f[i] = __uint_as_float(v[2 * i]) + __uint_as_float(v[2 * i + 1]);
    float s = ((f[0] + f[1]) + (f[2] + f[3])) + ((f[4] + f[5]) + (f[6] + f[7]));
    s = rbf(xrows_sum(s));
    const float r = xres + rbf(s * gate);
    if (lane < 16) out_[kvh * D + 16 * jj + lane] = (u16)f2bf(r);
    if (bid == 0 && lane == 0) __hip_atomic_store((GAS u32*)(ws + WS_EPOCH), tag, RLX_AGENT);
  }
  STAMP(8);
}

extern "C" void kernel_launch(void* const* d_in, const int* in_sizes, int n_in, void* d_out, int out_size, void* d_ws,
                              size_t ws_size, hipStream_t stream) {
  (void)hipFuncSetAttribute((const void*)fused_decode, hipFuncAttributeMaxDynamicSharedMemorySize, L_BYTES);
  fused_decode<<<256, NTHR, L_BYTES, stream>>>((const u16*)d_in[0], (const u16*)d_in[1], (const u16*)d_in[2], (const u16*)d_in[3],
                                               (const u16*)d_in[4], (const u16*)d_in[6], (const u16*)d_in[7], (u16*)d_out,
                                               (unsigned char*)d_ws);
}
```

```cpp
#include <hip/hip_runtime.h>
#include <hip/hip_bf16.h>
#include <stdint.h>

typedef unsigned u32;
typedef unsigned long long u64;
typedef unsigned short u16;
typedef unsigned u32x4 __attribute__((ext_vector_type(4)));
typedef unsigned u32x2 __attribute__((ext_vector_type(2)));
typedef float f32x4 __attribute__((ext_vector_type(4)));
typedef float f32x2 __attribute__((ext_vector_type(2)));
typedef short s16x4 __attribute__((ext_vector_type(4)));
typedef short s16x8 __attribute__((ext_vector_type(8)));
typedef __attribute__((ext_vector_type(2))) __bf16 bf16x2;
typedef __attribute__((ext_vector_type(8))) __bf16 bf16x8;
#define GAS __attribute__((address_space(1)))
#define LAS __attribute__((address_space(3)))
#define RLX_AGENT __ATOMIC_RELAXED, __HIP_MEMORY_SCOPE_AGENT

#define WIN 44
#define WAUX 2
#define H0_DELAY 100
#define HINT_G 20
#define HINT_C 0
#define SKIP 0
#define STAMPS 0

constexpr int B = 8, D = 448, HQ = 16, HKV = 8, SK = 4096, DH = 256, DFF = 1792;
constexpr int NC = 8, NTHR = (NC + 2) * 64;
constexpr int NSLOT = 32, NGRP = 22;
constexpr float CEXP = 0.0625f * 1.4426950408889634f;
constexpr unsigned SPIN_CAP = 1u << 18;

constexpr u32 WS_EPOCH = 0;
constexpr u32 WS_H0 = 65536;
constexpr u32 WS_ML = WS_H0 + 64 * 256 * 8;
constexpr u32 WS_HINT = WS_ML + 64 * 16 * 8;
constexpr u32 WS_H1 = WS_HINT + 64 * 256 * 8;
constexpr u32 WS_HF = WS_H1 + 32 * 8 * 3 * 128 * 8;
constexpr u32 WS_H2 = WS_HF + 4 * 28 * 56 * 32 * 8;
constexpr u32 WS_ST = WS_H2 + 4 * 28 * 32 * 32 * 8;

constexpr int L_RING = 0;
constexpr int L_Q = NSLOT * 4096;
constexpr int L_SC = L_Q + 1024;
constexpr int L_MX = L_SC + 8 * 1408;
constexpr int L_CTL = L_MX + 128;
constexpr int XN_STRIDE = 912, HA_STRIDE = 80, OA_STRIDE = 272;
constexpr int S_WQ = 0;
constexpr int S_W1 = S_WQ + 57344;
constexpr int S_W2 = S_W1 + 28672;
constexpr int S_XN = S_W2 + 28672;
constexpr int S_HA = S_XN + 8 * XN_STRIDE;
constexpr int S_OA = S_HA + 8 * HA_STRIDE;
constexpr int S_TAG = S_OA + 8 * OA_STRIDE;
constexpr int L_BYTES = L_CTL + 64 + 128;
static_assert(L_BYTES <= 160 * 1024 && S_TAG + 16 <= L_BYTES, "LDS");

__device__ __forceinline__ float bf2f(u32 b) { return __uint_as_float(b << 16); }
__device__ __forceinline__ u32 f2bf(float f) { __bf16 b = (__bf16)f; return (u32)__builtin_bit_cast(u16, b); }
__device__ __forceinline__ float rbf(float f) { return bf2f(f2bf(f)); }
__device__ __forceinline__ u32 pk2(float lo, float hi) {
  bf16x2 v = {(__bf16)lo, (__bf16)hi};
  return __builtin_bit_cast(u32, v);
}
__device__ __forceinline__ float dot2(u32 a, u32 b, float acc) {
  return __builtin_amdgcn_fdot2_f32_bf16(__builtin_bit_cast(bf16x2, a), __builtin_bit_cast(bf16x2, b), acc, false);
}
template <int CTRL> __device__ __forceinline__ float dpp(float x) {
  return __builtin_bit_cast(float, __builtin_amdgcn_mov_dpp(__builtin_bit_cast(int, x), CTRL, 0xf, 0xf, true));
}
template <int CTRL, int BANK> __device__ __forceinline__ float dppm(float old, float x) {
  return __builtin_bit_cast(float, __builtin_amdgcn_update_dpp(__builtin_bit_cast(int, old), __builtin_bit_cast(int, x), CTRL, 0xf, BANK, false));
}
constexpr int XOR1 = 0xB1, XOR2 = 0x4E, ROR4 = 0x124, ROR8 = 0x128, SHL4 = 0x104, SHR4 = 0x114, BCAST = 0x150;
__device__ __forceinline__ f32x2 swap16(float a, float b) {
  auto s = __builtin_amdgcn_permlane16_swap(__float_as_uint(a), __float_as_uint(b), false, false);
  return f32x2{__uint_as_float(s[0]), __uint_as_float(s[1])};
}
__device__ __forceinline__ f32x2 swap32(float a, float b) {
  auto s = __builtin_amdgcn_permlane32_swap(__float_as_uint(a), __float_as_uint(b), false, false);
  return f32x2{__uint_as_float(s[0]), __uint_as_float(s[1])};
}
__device__ __forceinline__ float xrows_sum(float x) {
  f32x2 s = swap16(x, x); x = s[0] + s[1];
  f32x2 t = swap32(x, x); return t[0] + t[1];
}
__device__ __forceinline__ float wave_sum(float x) {
  x += dpp<XOR1>(x); x += dpp<XOR2>(x); x += dpp<ROR4>(x); x += dpp<ROR8>(x);
  return xrows_sum(x);
}
typedef __amdgpu_buffer_rsrc_t rsrc_t;
__device__ __forceinline__ rsrc_t mk_rsrc(const void* p, u32 bytes) { return __builtin_amdgcn_make_buffer_rsrc((void*)p, 0, (int)bytes, 0x00020000); }
__device__ __forceinline__ u32x4 ldw(rsrc_t r, u32 voff, u32 soff) { return __builtin_amdgcn_raw_buffer_load_b128(r, voff, soff, WAUX); }
#define LGKM0() asm volatile("s_waitcnt lgkmcnt(0)" ::: "memory")
__device__ __forceinline__ void wg_barrier() {
  LGKM0();
  __builtin_amdgcn_s_barrier();
  asm volatile("" ::: "memory");
}
__device__ __forceinline__ void st_gran(GAS u64* g, u32 tag, u32 v) { __hip_atomic_store(g, ((u64)tag << 32) | (u64)v, RLX_AGENT); }
__device__ __forceinline__ u64 ld_gran(const GAS u64* g) { return __hip_atomic_load(g, RLX_AGENT); }
#define PSLEEP 1
__device__ __forceinline__ void lds_wait_ge(volatile LAS u32* p, u32 need) {
  for (unsigned spins = 0; (u32)__builtin_amdgcn_readfirstlane(*p) < need && spins < (SPIN_CAP << 4); ++spins) __builtin_amdgcn_s_sleep(PSLEEP);
  asm volatile("" ::: "memory");
}
#define PIN8(a) asm volatile("" : "+v"(a[0]), "+v"(a[1]), "+v"(a[2]), "+v"(a[3]), "+v"(a[4]), "+v"(a[5]), "+v"(a[6]), "+v"(a[7]))
#define PIN4(a) asm volatile("" : "+v"(a[0]), "+v"(a[1]), "+v"(a[2]), "+v"(a[3]) : : "memory")

#define STAMP(i) do { } while (0)

__device__ __forceinline__ float k_unit(const u32x4 (&k)[4], const u32x4& q0, const u32x4& q1) {
  float e[4];
#pragma unroll
  for (int i = 0; i < 4; ++i) {
    float a = 0.f, b = 0.f;
#pragma unroll
    for (int j = 0; j < 4; ++j) { a = dot2(k[i][j], q0[j], a); b = dot2(k[i][j], q1[j], b); }
    const f32x2 s = swap16(a, b);
    e[i] = s[0] + s[1];
  }
  const float f0 = dppm<ROR8, 0x3>(e[2], e[0]) + dppm<ROR8, 0xc>(e[0], e[2]);
  const float f1 = dppm<ROR8, 0x3>(e[3], e[1]) + dppm<ROR8, 0xc>(e[1], e[3]);
  float g = dppm<SHL4, 0x5>(f1, f0) + dppm<SHR4, 0xa>(f0, f1);
  g += dpp<XOR1>(g); g += dpp<XOR2>(g);
  return rbf(g);
}
__device__ __forceinline__ void v_piece(f32x2 (&acc)[8], float p0, float p1, u32x4 v) {
#pragma unroll
  for (int j = 0; j < 4; ++j) {
    const f32x2 vv = {__uint_as_float(v[j] << 16), __uint_as_float(v[j] & 0xffff0000u)};
    acc[j] = __builtin_elementwise_fma(vv, f32x2{p0, p0}, acc[j]);
    acc[4 + j] = __builtin_elementwise_fma(vv, f32x2{p1, p1}, acc[4 + j]);
  }
}

#define DMA_W(R, VO, SO, M0) asm volatile("s_mov_b32 m0, %0\n\ts_nop 0\n\tbuffer_load_dwordx4 %1, %2, %3 offen nt lds" :: "s"(M0), "v"(VO), "s"(R), "s"(SO) : "m0", "memory")
#define VM0() asm volatile("s_waitcnt vmcnt(0)" ::: "memory")
__device__ __forceinline__ s16x8 tr_frag(const LAS unsigned char* p, int four_rows) {
  const s16x4 b0 = __builtin_amdgcn_ds_read_tr16_b64_v4i16((LAS s16x4*)p);
  const s16x4 b1 = __builtin_amdgcn_ds_read_tr16_b64_v4i16((LAS s16x4*)(p + four_rows));
  return s16x8{b0[0], b0[1], b0[2], b0[3], b1[0], b1[1], b1[2], b1[3]};
}
__device__ __forceinline__ f32x4 mfma16(s16x8 a, s16x8 b, f32x4 c) {
  return __builtin_amdgcn_mfma_f32_16x16x32_bf16(__builtin_bit_cast(bf16x8, a), __builtin_bit_cast(bf16x8, b), c, 0, 0, 0);
}
template <int KS> __device__ __forceinline__ f32x4 gemm8x16(const LAS unsigned char* A, int astride, const LAS unsigned char* Bt, int bstride, int lane) {
  f32x4 acc = {0.f, 0.f, 0.f, 0.f};
  const int g = lane >> 4;
  const LAS unsigned char* ap = A + (lane & 7) * astride + 16 * g;
  const LAS unsigned char* bp = Bt + (8 * g + ((lane & 15) >> 2)) * bstride + 8 * (lane & 3);
#pragma unroll
  for (int ks = 0; ks < KS; ++ks) acc = mfma16(*(const LAS s16x8*)(ap + 64 * ks), tr_frag(bp + ks * 32 * bstride, 4 * bstride), acc);
  return acc;
}
__device__ __forceinline__ u32 pair_of(f32x4 acc, int lane, int& batch) {
  const int g = lane >> 4;
  const float lo = (g >> 1) ? acc[2] : acc[0], hi = (g >> 1) ? acc[3] : acc[1];
  const float send = (lane & 1) ? lo : hi, keep = (lane & 1) ? hi : lo;
  const float got = dpp<XOR1>(send);
  batch = 4 * (g & 1) + (g >> 1) * 2 + (lane & 1);
  return (lane & 1) ? pk2(got, keep) : pk2(keep, got);
}
__device__ __forceinline__ void st_gran2(rsrc_t wsr, u32 voff, u32 soff, u32 tag, float a, float b) {
  __builtin_amdgcn_raw_buffer_store_b128(u32x4{__float_as_uint(a), tag, __float_as_uint(b), tag}, wsr, voff + soff, 0, 16);
}
__device__ __forceinline__ void pub_tile(rsrc_t wsr, u32 soff, u32 pstride, f32x4 acc, u32 tag, int lane) {
  const int g = lane >> 4;
  st_gran2(wsr, (u32)(2 * (g & 1) + (g >> 1)) * pstride + (lane & 15) * 16, soff, tag, (g >> 1) ? acc[2] : acc[0], (g >> 1) ? acc[3] : acc[1]);
}
__device__ __forceinline__ void nap(int units) {
  for (int i = 0; i < units; ++i) __builtin_amdgcn_s_sleep(127);
}
template <int N, int SLEEP> __device__ __forceinline__ void sweep1(rsrc_t wsr, u32 voff, u32 soff, u32 stride, u32 tag, u32 (&v)[N]) {
  unsigned have = 0;
#pragma unroll
  for (int i = 0; i < N; ++i) v[i] = 0u;
  for (unsigned spins = 0;; ++spins) {
    u32x2 t[N];
    asm volatile("" ::: "memory");
#pragma unroll
    for (int i = 0; i < N; ++i) t[i] = __builtin_amdgcn_raw_buffer_load_b64(wsr, voff, ((have >> i) & 1u) ? 0x40000000u : soff + i * stride, 16);
#pragma unroll
    for (int i = 0; i < N; ++i) {
      v[i] = ((have >> i) & 1u) ? v[i] : t[i][0];
      have |= __all(t[i][1] == tag) ? 1u << i : 0u;
    }
    if (have == (1u << N) - 1u || spins > SPIN_CAP) break;
    __builtin_amdgcn_s_sleep(SLEEP);
  }
  asm volatile("" ::: "memory");
}
template <int N, int SLEEP> __device__ __forceinline__ void sweep2(rsrc_t wsr, u32 voff, u32 soff, u32 stride, u32 tag, f32x2 (&v)[N]) {
  unsigned have = 0;
#pragma unroll
  for (int i = 0; i < N; ++i) v[i] = f32x2{0.f, 0.f};
  for (unsigned spins = 0;; ++spins) {
    u32x4 t[N];
    asm volatile("" ::: "memory");
#pragma unroll
    for (int i = 0; i < N; ++i) t[i] = __builtin_amdgcn_raw_buffer_load_b128(wsr, voff, ((have >> i) & 1u) ? 0x40000000u : soff + i * stride, 16);
#pragma unroll
    for (int i = 0; i < N; ++i) {
      v[i] = ((have >> i) & 1u) ? v[i] : f32x2{__uint_as_float(t[i][0]), __uint_as_float(t[i][2])};
      have |= __all(t[i][1] == tag && t[i][3] == tag) ? 1u << i : 0u;
    }
    if (have == (1u << N) - 1u || spins > SPIN_CAP) break;
    __builtin_amdgcn_s_sleep(SLEEP);
  }
  asm volatile("" ::: "memory");
}

__global__ void __launch_bounds__(NTHR, 3)
fused_decode(const u16* __restrict__ x_, const u16* __restrict__ kc_, const u16* __restrict__ vc_, const u16* __restrict__ wq_,
             const u16* __restrict__ wo_, const u16* __restrict__ w1_, const u16* __restrict__ w2_, u16* __restrict__ out_,
             unsigned char* ws_) {
  extern __shared__ __attribute__((aligned(16))) unsigned char smem[];
  LAS unsigned char* L = (LAS unsigned char*)smem;
  GAS unsigned char* ws = (GAS unsigned char*)ws_;
  const int tid = threadIdx.x, lane = tid & 63, wave = __builtin_amdgcn_readfirstlane(tid >> 6);
  const int bid = blockIdx.x, kvh = bid & 7, jj = bid >> 3;
  const u32 ldsb = (u32)(uintptr_t)L;
  const rsrc_t wsr = mk_rsrc(ws_, WS_ST);

  nap(5);
  if (jj >= 24) {
    const int t = jj - 24, c = wave - 1;
    const bool mid = wave >= 1 && wave <= 8;
    if (wave == 0) STAMP(0);
    u32 tag;
    if (mid) {
      u32x4 xw;
      asm volatile("global_load_dwordx4 %0, %1, off" : "=v"(xw) : "v"(x_ + c * D + 8 * (lane < 56 ? lane : 55)) : "memory");
      {
        const rsrc_t r = mk_rsrc(wq_, (SKIP & 8) ? 0u : (u32)D * HQ * DH * 2);
        const u32 vo = (lane >> 3) * (HQ * DH * 2) + (lane & 7) * 16;
#pragma unroll
        for (int it = 0; it < 7; ++it) DMA_W(r, vo, (56 * c + 8 * it) * (HQ * DH * 2) + kvh * 1024 + t * 128, ldsb + S_WQ + (7 * c + it) * 1024);
      }
      asm volatile("s_waitcnt vmcnt(7)" : "+v"(xw) :: "memory");
      float xv[8];
#pragma unroll
      for (int j = 0; j < 4; ++j) { xv[2 * j] = __uint_as_float(xw[j] << 16); xv[2 * j + 1] = __uint_as_float(xw[j] & 0xffff0000u); }
      float s = ((xv[0] + xv[1]) + (xv[2] + xv[3])) + ((xv[4] + xv[5]) + (xv[6] + xv[7]));
      const float mean = wave_sum(lane < 56 ? s : 0.f) * (1.f / D);
      float ss = 0.f;
#pragma unroll
      for (int j = 0; j < 8; ++j) { const float d = xv[j] - mean; ss = fmaf(d, d, ss); }
      const float var = rbf(wave_sum(lane < 56 ? ss : 0.f) * (1.f / D));
      const float mb = rbf(mean), sd = rbf(sqrtf(rbf(var + 1e-5f)));
      u32x4 o;
#pragma unroll
      for (int j = 0; j < 4; ++j) o[j] = pk2(rbf(xv[2 * j] - mb) / sd, rbf(xv[2 * j + 1] - mb) / sd);
      if (lane < 56) *(LAS u32x4*)(L + S_XN + c * XN_STRIDE + 16 * lane) = o;
      VM0();
    } else {
      if (wave == 0) {
        const u32 e = __hip_atomic_load((const GAS u32*)(ws + WS_EPOCH), RLX_AGENT);
        if (lane == 0) *(LAS u32*)(L + S_TAG) = e + 1u;
      }
    }
    wg_barrier();
    tag = __builtin_amdgcn_readfirstlane(*(const LAS u32*)(L + S_TAG));
    if (wave >= 1 && wave <= 4) {
      const f32x4 acc = gemm8x16<14>(L + S_XN, XN_STRIDE, L + S_WQ + 32 * c, 128, lane);
      int bb; const u32 v = pair_of(acc, lane, bb);
      st_gran((GAS u64*)(ws + WS_H0) + (size_t)(bb * 8 + kvh) * 256 + 32 * t + 8 * c + ((lane & 15) >> 1), tag, v);
      if (wave == 1) STAMP(9);
    } else if (t < 7) {
      const int w = wave == 0 ? 0 : wave - 4;
      const rsrc_t r1 = mk_rsrc(w1_, (u32)D * DFF * 2), r2 = mk_rsrc(w2_, (u32)D * DFF * 2);
      const u32 vo = (lane >> 2) * (DFF * 2) + (lane & 3) * 16;
      for (int it = w; it < ((SKIP & 1) ? 0 : 28); it += 6) DMA_W(r1, vo, it * 16 * (DFF * 2) + kvh * 448 + t * 64, ldsb + S_W1 + it * 1024);
      for (int it = w; it < ((SKIP & 1) ? 0 : 28); it += 6) DMA_W(r2, (u32)lane * 16, (kvh * 224 + t * 32) * (D * 2) + it * 1024, ldsb + S_W2 + it * 1024);
      VM0();
    }
    wg_barrier();
    if (mid) {
      const rsrc_t r = mk_rsrc(wo_, (u32)D * HQ * DH * 2);
#pragma unroll
      for (int it = 0; it < ((SKIP & 4) ? 0 : 7); ++it) {
        const int i = lane + 64 * (7 * c + it), row = i / 28;
        DMA_W(r, (u32)(row * (D * 2) + (i - row * 28) * 16), (4 * kvh + (t >> 1)) * 128 * (D * 2) + (t & 1) * 448, ldsb + S_WQ + (7 * c + it) * 1024);
      }
    } else if (t < 7) {
      const int half = wave != 0;
      f32x4 acc = gemm8x16<14>(L + S_XN, XN_STRIDE, L + S_W1 + 32 * half, 64, lane);
#pragma unroll
      for (int i = 0; i < 4; ++i) acc[i] = fmaxf(acc[i], 0.f);
      int bb; const u32 v = pair_of(acc, lane, bb);
      *(LAS u32*)(L + S_HA + bb * HA_STRIDE + half * 32 + 4 * ((lane & 15) >> 1)) = v;
    }
    wg_barrier();
    if (t < 7 && wave >= 1 && wave <= 7 && !(SKIP & 2)) {
#pragma unroll
      for (int i = 0; i < 4; ++i) {
        const f32x4 acc = gemm8x16<1>(L + S_HA, HA_STRIDE, L + S_W2 + 32 * (4 * c + i), D * 2, lane);
        pub_tile(wsr, WS_HF + ((4 * c + i) * 56 + kvh * 7 + t) * 256, 28 * 56 * 256, acc, tag, lane);
      }
    }
    VM0();
    wg_barrier();
    if (mid) {
      s16x8 wb[2][4];
      {
        const LAS unsigned char* bp = L + S_WQ + (8 * (lane >> 4) + ((lane & 15) >> 2)) * 448 + 64 * (c < 7 ? c : 6) + 8 * (lane & 3);
#pragma unroll
        for (int i = 0; i < 2; ++i)
#pragma unroll
          for (int ks = 0; ks < 4; ++ks) wb[i][ks] = tr_frag(bp + 32 * i + ks * 32 * 448, 4 * 448);
        asm volatile("" : "+v"(wb[0][0]), "+v"(wb[0][1]), "+v"(wb[0][2]), "+v"(wb[0][3]), "+v"(wb[1][0]), "+v"(wb[1][1]), "+v"(wb[1][2]), "+v"(wb[1][3]));
      }
      float w0, w1, w2;
      {
        nap(5);
        u32 v[1];
        sweep1<1, 100>(wsr, (u32)((lane & 15) < 12 ? (lane & 15) : 0) * 8, WS_ML + (c * 8 + kvh) * 128, 0, tag, v);
        const int h = t >> 2;
        float mv[3], lv[3];
#pragma unroll
        for (int s = 0; s < 3; ++s) {
          mv[s] = __uint_as_float(__builtin_amdgcn_readlane(v[0], 4 * s + h));
          lv[s] = __uint_as_float(__builtin_amdgcn_readlane(v[0], 4 * s + 2 + h));
        }
        const float M = fmaxf(fmaxf(mv[0], mv[1]), mv[2]);
        w0 = __builtin_amdgcn_exp2f((mv[0] - M) * CEXP); w1 = __builtin_amdgcn_exp2f((mv[1] - M) * CEXP); w2 = __builtin_amdgcn_exp2f((mv[2] - M) * CEXP);
        const float Ls = fmaf(w0, lv[0], fmaf(w1, lv[1], w2 * lv[2]));
        w0 /= Ls; w1 /= Ls; w2 /= Ls;
      }
      if (c == 0) STAMP(10);
      {
        u32 hv[1];
        sweep1<1, 16>(wsr, (u32)((lane & 3) < 3 ? (lane & 3) : 0) * 8, WS_HINT + (kvh * 8 + t) * 2048 + (kvh * 24 + c * 3) * 8, 0, tag, hv);
        f32x2 v[3];
        sweep2<3, 1>(wsr, lane * 16, WS_H1 + ((4 * kvh + (t >> 1)) * 8 + c) * 3072, 1024, tag, v);
        *(LAS u32*)(L + S_OA + c * OA_STRIDE + 4 * lane) =
            pk2(fmaf(w0, v[0][0], fmaf(w1, v[1][0], w2 * v[2][0])), fmaf(w0, v[0][1], fmaf(w1, v[1][1], w2 * v[2][1])));
      }
      if (c == 7) STAMP(6);
      wg_barrier();
      if (c < 7) {
        const LAS unsigned char* ap = L + S_OA + (lane & 7) * OA_STRIDE + 16 * (lane >> 4);
        f32x4 s0 = {0.f, 0.f, 0.f, 0.f}, s1 = {0.f, 0.f, 0.f, 0.f};
#pragma unroll
        for (int ks = 0; ks < 4; ++ks) { const s16x8 a = *(const LAS s16x8*)(ap + 64 * ks); s0 = mfma16(a, wb[0][ks], s0); s1 = mfma16(a, wb[1][ks], s1); }
        const u32 so = WS_H2 + ((14 * (t & 1) + 2 * c) * 32 + 4 * kvh + (t >> 1)) * 256;
        pub_tile(wsr, so, 28 * 32 * 256, s0, tag, lane);
        pub_tile(wsr, so + 32 * 256, 28 * 32 * 256, s1, tag, lane);
        if (c == 0) STAMP(21);
        if (STAMPS) { VM0(); if (c == 0) STAMP(22); }
      }
      return;
    }
    if (t == 7) return;
    const int f = 2 * (7 * kvh + t) + (wave != 0), bp = f / 28, ct = f - bp * 28;
    f32x2 gate, xres;
    {
      const GAS u16* xp = (const GAS u16*)x_ + 2 * bp * D + 16 * ct + (lane & 15);
      xres = f32x2{bf2f(xp[0]), bf2f(xp[D])};
      nap(1);
      f32x2 v[14];
      if (SKIP & 2) { for (int i = 0; i < 14; ++i) v[i] = xres; } else
      sweep2<14, 100>(wsr, lane * 16, WS_HF + f * (56 * 256), 1024, tag, v);
      f32x2 s = ((v[0] + v[1]) + (v[2] + v[3])) + ((v[4] + v[5]) + (v[6] + v[7])) + (((v[8] + v[9]) + (v[10] + v[11])) + (v[12] + v[13]));
      const float g0 = rbf(xrows_sum(s[0])), g1 = rbf(xrows_sum(s[1]));
      gate = f32x2{rbf(g0 / (1.f + __expf(-g0))), rbf(g1 / (1.f + __expf(-g1)))};
    }
    if (wave == 0) STAMP(5);
    nap(4);
    { u32 hv[3]; sweep1<3, 32>(wsr, lane * 8, WS_HINT + (kvh * 8 + t) * 2048, 512, tag, hv); }
    wg_barrier();
    if (wave == 0) STAMP(7);
    {
      f32x2 v[8];
      sweep2<8, 1>(wsr, lane * 16, WS_H2 + f * (32 * 256), 1024, tag, v);
      if (wave == 0) STAMP(23);
      const f32x2 s = ((v[0] + v[1]) + (v[2] + v[3])) + ((v[4] + v[5]) + (v[6] + v[7]));
      const f32x2 u = swap16(s[0], s[1]);
      const float h = u[0] + u[1];
      const f32x2 w = swap32(h, h);
      const float sv = rbf(w[0] + w[1]);
      const int odd = (lane >> 4) & 1;
      const float r = (odd ? xres[1] : xres[0]) + rbf(sv * (odd ? gate[1] : gate[0]));
      if (lane < 32) out_[(2 * bp + odd) * D + 16 * ct + (lane & 15)] = (u16)f2bf(r);
      if (f == 0 && lane == 0) __hip_atomic_store((GAS u32*)(ws + WS_EPOCH), tag, RLX_AGENT);
    }
    if (wave == 0) STAMP(8);
    return;
  }

  volatile LAS u32* ctl = (volatile LAS u32*)(L + L_CTL);
  const int b = jj / 3, sp = jj - 3 * b, pair = b * 8 + kvh;
  const int nu = sp == 2 ? 170 : 171;

  if (wave == 0) {
    __builtin_amdgcn_s_setprio(3);
    STAMP(0);
    const rsrc_t kr = mk_rsrc(kc_, (u32)B * HKV * SK * DH * 2), vr = mk_rsrc(vc_, (u32)B * HKV * SK * DH * 2);
    const u32 so0 = (u32)pair * (SK * DH * 2) + (u32)sp * (171 * 4096), vo = lane * 16;
    constexpr int WU = WIN / 4;
#define DMA_UNIT(R, SO, M0, PUB) asm volatile("ds_read_b32 %0, %8\n\ts_mov_b32 m0, %1\n\t" \
      "s_waitcnt vmcnt(%5)\n\tbuffer_load_dwordx4 %2, %3, %4 offen nt lds\n\t" \
      "s_waitcnt vmcnt(%5)\n\tbuffer_load_dwordx4 %2, %3, %4 offen offset:1024 nt lds\n\t" \
      "s_waitcnt vmcnt(%5)\n\tbuffer_load_dwordx4 %2, %3, %4 offen offset:2048 nt lds\n\t" \
      "s_waitcnt vmcnt(%5)\n\tbuffer_load_dwordx4 %2, %3, %4 offen offset:3072 nt lds\n\t" \
      "s_waitcnt lgkmcnt(0)\n\tds_write_b32 %6, %7" \
      : "=&v"(dn) : "s"(M0), "v"(vo), "s"(R), "s"(SO), "n"(WIN - 1), "v"(pubaddr), "v"(PUB), "v"(dnaddr) : "m0", "memory")
    u32 dn;
    const u32 dnaddr = ldsb + L_CTL + 32 + 4 * (lane & 7);
    {
      const u32 pubaddr = ldsb + L_CTL + 28;
#pragma unroll
      for (int u = 0; u < WU; ++u) DMA_UNIT(kr, so0 + u * 4096, ldsb + u * 4096, 0u);
    }
    wg_barrier();
    const u32 pubaddr = ldsb + L_CTL;
    dn = 0u;
#pragma unroll
    for (int u = WU; u < 16; ++u) DMA_UNIT(kr, so0 + u * 4096, ldsb + u * 4096, (u32)(u - WU + 1));
    u32 pub = 0, iss = 16;
    for (int g = 2; g < 2 * NGRP; ++g) {
      const int gk = g < NGRP ? g : g - NGRP, cnt = gk < NGRP - 1 ? 8 : nu - 8 * (NGRP - 1);
      const int need = g - NSLOT / 8 + 1;
      u32 so = so0 + (u32)gk * 32768, m0 = ldsb + (u32)(g & (NSLOT / 8 - 1)) * 32768;
      const rsrc_t rr = g < NGRP ? kr : vr;
#pragma unroll 1
      for (int i = 0; i < cnt; ++i) {
        if ((int)__builtin_amdgcn_readlane(dn, i) < need) {
          VM0();
          pub = iss;
          ctl[0] = pub;
          lds_wait_ge(ctl + 8 + i, (u32)need);
        }
        const u32 pv = iss - WU + 1;
        pub = pub > pv ? pub : pv;
        DMA_UNIT(rr, so, m0, pub);
        so += 4096; m0 += 4096; ++iss;
      }
    }
#pragma unroll
    for (int r = WU - 1; r >= 0; --r) {
      asm volatile("s_waitcnt vmcnt(%0)" :: "n"(4 * r) : "memory");
      pub = pub > iss - r ? pub : iss - r;
      ctl[0] = pub;
    }
    STAMP(2);
    return;
  }

  if (wave == NC + 1) {
    u32 tag = __hip_atomic_load((const GAS u32*)(ws + WS_EPOCH), RLX_AGENT);
    if (lane < 16) ctl[lane] = 0u;
    wg_barrier();
    asm volatile("" : "+v"(tag));
    tag = __builtin_amdgcn_readfirstlane(tag) + 1u;
    ctl[3] = tag;
    __builtin_amdgcn_s_sleep(H0_DELAY);
    f32x2 v[2];
    if (SKIP & 16) { v[0] = v[1] = f32x2{1e-3f, 1e-3f}; } else
    sweep2<2, 16>(wsr, lane * 16, WS_H0 + pair * 2048, 1024, tag, v);
    *(LAS f32x2*)(L + L_Q + 8 * lane) = v[0];
    *(LAS f32x2*)(L + L_Q + 512 + 8 * lane) = v[1];
    LGKM0();
    ctl[2] = 1u;
    STAMP(1);
    return;
  }

  const int c = wave - 1, cl = lane & 31;
  wg_barrier();
  const LAS unsigned char* rp = L + L_RING + c * 4096 + lane * 16;
#define TAKE(dst, g, need) do { lds_wait_ge(ctl, (u32)(need)); \
    _Pragma("unroll") for (int i_ = 0; i_ < 4; ++i_) dst[i_] = *(const LAS u32x4*)(rp + ((g) & 3) * 32768 + i_ * 1024); \
    LGKM0(); ctl[8 + c] = (u32)((g) + 1); PIN4(dst); } while (0)
  LAS float* scp = (LAS float*)(L + L_SC + c * 1408);
  LAS float* scw = scp + (lane >> 5) * 8 + ((lane >> 2) & 3) * 2 + ((lane >> 4) & 1);
  const bool last = 8 * (NGRP - 1) + c < nu;
  u32x4 q0, q1;
  float m;
  int g0 = 0;
  {
    u32x4 k0[4], k1[4], k2[4], k3[4], k4[4], k5[4], k6[4], k7[4];
#define PRE(kk, i) if (g0 == (i) && __builtin_amdgcn_readfirstlane(ctl[2]) == 0u) { TAKE(kk, i, c + 8 * (i) + 1); g0 = (i) + 1; }
    PRE(k0, 0) PRE(k1, 1) PRE(k2, 2) PRE(k3, 3) PRE(k4, 4) PRE(k5, 5) PRE(k6, 6) PRE(k7, 7)
    lds_wait_ge(ctl + 2, 1u);
    q0 = *(const LAS u32x4*)(L + L_Q + 16 * cl); q1 = *(const LAS u32x4*)(L + L_Q + 512 + 16 * cl);
    scw[16 * (NGRP - 1)] = -INFINITY;
    m = -INFINITY;
#define POST(kk, i) if (g0 > (i)) { const float s_ = k_unit(kk, q0, q1); scw[16 * (i)] = s_; m = fmaxf(m, s_); }
    POST(k0, 0) POST(k1, 1) POST(k2, 2) POST(k3, 3) POST(k4, 4) POST(k5, 5) POST(k6, 6) POST(k7, 7)
  }
  const int ng = last ? NGRP : NGRP - 1;
#pragma unroll 1
  for (int g = g0; g < ng; ++g) {
    u32x4 k[4];
    TAKE(k, g, 8 * g + c + 1);
    const float s = k_unit(k, q0, q1);
    scw[16 * g] = s;
    m = fmaxf(m, s);
  }
  ctl[8 + c] = (u32)NGRP;
  {
    m = fmaxf(m, dpp<ROR4>(m)); m = fmaxf(m, dpp<ROR8>(m));
    { const f32x2 t = swap32(m, m); m = fmaxf(t[0], t[1]); }
    if ((lane & 47) == 0) ((LAS float*)(L + L_MX))[2 * c + (lane >> 4)] = m;
    LGKM0();
    if (lane == 0) __hip_atomic_fetch_add((LAS u32*)(L + L_CTL + 4), 1u, __ATOMIC_RELAXED, __HIP_MEMORY_SCOPE_WORKGROUP);
    lds_wait_ge(ctl + 1, 8u);
    float M = ((const LAS float*)(L + L_MX))[lane & 15];
    M = fmaxf(M, dpp<XOR2>(M)); M = fmaxf(M, dpp<ROR4>(M)); M = fmaxf(M, dpp<ROR8>(M));
    const float M0 = dpp<0xA0>(M), M1 = dpp<0xF5>(M);
    float l0 = 0.f, l1 = 0.f;
#pragma unroll
    for (int i = 0; i < 2; ++i) {
      if (i == 0 || lane < 24) {
        f32x4 s = *(const LAS f32x4*)(scp + 4 * (lane + 64 * i));
        s[0] = __builtin_amdgcn_exp2f((s[0] - M0) * CEXP); s[1] = __builtin_amdgcn_exp2f((s[1] - M1) * CEXP);
        s[2] = __builtin_amdgcn_exp2f((s[2] - M0) * CEXP); s[3] = __builtin_amdgcn_exp2f((s[3] - M1) * CEXP);
        *(LAS f32x4*)(scp + 4 * (lane + 64 * i)) = s;
        l0 += s[0] + s[2]; l1 += s[1] + s[3];
      }
    }
    l0 = wave_sum(l0); l1 = wave_sum(l1);
    if (lane == 0) *(LAS f32x2*)(L + L_MX + 64 + 8 * c) = f32x2{l0, l1};
    LGKM0();
    u32 old = 0;
    if (lane == 0) old = __hip_atomic_fetch_add((LAS u32*)(L + L_CTL + 16), 1u, __ATOMIC_RELAXED, __HIP_MEMORY_SCOPE_WORKGROUP);
    old = __builtin_amdgcn_readfirstlane(old);
    asm volatile("" ::: "memory");
    if (old == 7u) {
      float Ls = ((const LAS float*)(L + L_MX + 64))[lane & 15];
      Ls += dpp<XOR2>(Ls); Ls += dpp<ROR4>(Ls); Ls += dpp<ROR8>(Ls);
      if (lane < 4) st_gran((GAS u64*)(ws + WS_ML) + pair * 16 + sp * 4 + lane, __builtin_amdgcn_readfirstlane(ctl[3]), __float_as_uint((lane & 2) ? Ls : M));
    }
  }
  f32x2 acc[8];
#pragma unroll
  for (int j = 0; j < 8; ++j) acc[j] = f32x2{0.f, 0.f};
  const LAS float* scr = scp + (lane >> 5) * 8;
  const u32 tag = __builtin_amdgcn_readfirstlane(ctl[3]);
#pragma unroll 1
  for (int g = 0; g < ng; ++g) {
    const f32x4 P0 = *(const LAS f32x4*)(scr + 16 * g), P1 = *(const LAS f32x4*)(scr + 16 * g + 4);
    u32x4 v[4];
    TAKE(v, NGRP + g, nu + 8 * g + c + 1);
    if (c == HINT_C && g == HINT_G) st_gran((GAS u64*)(ws + WS_HINT) + lane * 256 + kvh * 24 + jj, tag, 1u);
    v_piece(acc, P0[0], P0[1], v[0]); v_piece(acc, P0[2], P0[3], v[1]);
    v_piece(acc, P1[0], P1[1], v[2]); v_piece(acc, P1[2], P1[3], v[3]);
  }
  if (c == 1) STAMP(3);
  {
    float o[8];
#pragma unroll
    for (int j = 0; j < 8; ++j) { const f32x2 t = swap32(acc[j >> 1][j & 1], acc[4 + (j >> 1)][j & 1]); o[j] = t[0] + t[1]; }
    LAS float* dst = (LAS float*)(L + L_RING + c * 4096) + (lane >> 5) * 256 + 8 * cl;
    *(LAS f32x4*)dst = f32x4{o[0], o[1], o[2], o[3]};
    *(LAS f32x4*)(dst + 4) = f32x4{o[4], o[5], o[6], o[7]};
  }
  wg_barrier();
  if (c < 4) {
    const LAS f32x2* pp = (const LAS f32x2*)(L + L_RING) + c * 64 + lane;
    const f32x2 o = ((pp[0] + pp[512]) + (pp[1024] + pp[1536])) + ((pp[2048] + pp[2560]) + (pp[3072] + pp[3584]));
    st_gran2(wsr, lane * 16, WS_H1 + (((4 * kvh + c) * 8 + b) * 3 + sp) * 1024, tag, o[0], o[1]);
  }
  if (c == 1) STAMP(4);
}

extern "C" void kernel_launch(void* const* d_in, const int* in_sizes, int n_in, void* d_out, int out_size, void* d_ws,
                              size_t ws_size, hipStream_t stream) {
  (void)hipFuncSetAttribute((const void*)fused_decode, hipFuncAttributeMaxDynamicSharedMemorySize, L_BYTES);
  fused_decode<<<256, NTHR, L_BYTES, stream>>>((const u16*)d_in[0], (const u16*)d_in[1], (const u16*)d_in[2], (const u16*)d_in[3],
                                               (const u16*)d_in[4], (const u16*)d_in[6], (const u16*)d_in[7], (u16*)d_out,
                                               (unsigned char*)d_ws);
}
```

```cpp
#include <hip/hip_runtime.h>
#include <hip/hip_bf16.h>
#include <stdint.h>

typedef unsigned u32;
typedef unsigned long long u64;
typedef unsigned short u16;
typedef unsigned u32x4 __attribute__((ext_vector_type(4)));
typedef unsigned u32x2 __attribute__((ext_vector_type(2)));
typedef float f32x4 __attribute__((ext_vector_type(4)));
typedef float f32x2 __attribute__((ext_vector_type(2)));
typedef short s16x4 __attribute__((ext_vector_type(4)));
typedef short s16x8 __attribute__((ext_vector_type(8)));
typedef __attribute__((ext_vector_type(2))) __bf16 bf16x2;
typedef __attribute__((ext_vector_type(8))) __bf16 bf16x8;
#define GAS __attribute__((address_space(1)))
#define LAS __attribute__((address_space(3)))
#define RLX_AGENT __ATOMIC_RELAXED, __HIP_MEMORY_SCOPE_AGENT

#define WIN 20
#define WAUX 2
#define KVPOL "nt"
#define H0_AT 600
#define HINT_POS 1
#define SKIP 0
#define STAMPS 0

constexpr int B = 8, D = 448, HQ = 16, HKV = 8, SK = 4096, DH = 256, DFF = 1792;
constexpr int NC = 8, NTHR = (NC + 3) * 64;
constexpr int NSLOT = 32, NGRP = 22;
constexpr float CEXP = 0.0625f * 1.4426950408889634f;
constexpr unsigned SPIN_CAP = 1u << 18;

constexpr u32 WS_EPOCH = 0;
constexpr u32 WS_H0 = 65536;
constexpr u32 WS_ML = WS_H0 + 64 * 256 * 8;
constexpr u32 WS_N1 = WS_ML + 64 * 16 * 8;
constexpr u32 WS_N2 = WS_N1 + 64 * 32 * 8;
constexpr u32 WS_HH = WS_N2 + 112 * 16 * 8;
constexpr u32 WS_HF = WS_HH + 8 * 896 * 8;
constexpr u32 WS_H1 = WS_HF + 112 * 8 * 32 * 8;
constexpr u32 WS_H2 = WS_H1 + 16 * 8 * 3 * 256 * 8;
constexpr u32 WS_ST = WS_H2 + 112 * 16 * 32 * 8;

constexpr int L_RING = 0;
constexpr int L_Q = NSLOT * 4096;
constexpr int L_SC = L_Q + 1024;
constexpr int L_MX = L_SC + 8 * 1408;
constexpr int L_CTL = L_MX + 128;
constexpr int XN_STRIDE = 912, HA_STRIDE = 464, OA_STRIDE = 528;
constexpr int S_WQ = 0;
constexpr int S_W1 = S_WQ + 57344;
constexpr int S_W2 = S_W1 + 28672;
constexpr int S_XN = S_W2 + 28672;
constexpr int S_HA = S_XN + 8 * XN_STRIDE;
constexpr int S_OA = S_HA + 8 * HA_STRIDE;
constexpr int S_TAG = S_OA + 8 * OA_STRIDE;
constexpr int L_BYTES = L_CTL + 64 + 128;
static_assert(L_BYTES <= 160 * 1024 && S_TAG + 16 <= L_BYTES, "LDS");

__device__ __forceinline__ float bf2f(u32 b) { return __uint_as_float(b << 16); }
__device__ __forceinline__ u32 f2bf(float f) { __bf16 b = (__bf16)f; return (u32)__builtin_bit_cast(u16, b); }
__device__ __forceinline__ float rbf(float f) { return bf2f(f2bf(f)); }
__device__ __forceinline__ u32 pk2(float lo, float hi) {
  bf16x2 v = {(__bf16)lo, (__bf16)hi};
  return __builtin_bit_cast(u32, v);
}
__device__ __forceinline__ float dot2(u32 a, u32 b, float acc) {
  return __builtin_amdgcn_fdot2_f32_bf16(__builtin_bit_cast(bf16x2, a), __builtin_bit_cast(bf16x2, b), acc, false);
}
template <int CTRL> __device__ __forceinline__ float dpp(float x) {
  return __builtin_bit_cast(float, __builtin_amdgcn_mov_dpp(__builtin_bit_cast(int, x), CTRL, 0xf, 0xf, true));
}
template <int CTRL, int BANK> __device__ __forceinline__ float dppm(float old, float x) {
  return __builtin_bit_cast(float, __builtin_amdgcn_update_dpp(__builtin_bit_cast(int, old), __builtin_bit_cast(int, x), CTRL, 0xf, BANK, false));
}
constexpr int XOR1 = 0xB1, XOR2 = 0x4E, ROR4 = 0x124, ROR8 = 0x128, SHL4 = 0x104, SHR4 = 0x114, BCAST = 0x150;
__device__ __forceinline__ f32x2 swap16(float a, float b) {
  auto s = __builtin_amdgcn_permlane16_swap(__float_as_uint(a), __float_as_uint(b), false, false);
  return f32x2{__uint_as_float(s[0]), __uint_as_float(s[1])};
}
__device__ __forceinline__ f32x2 swap32(float a, float b) {
  auto s = __builtin_amdgcn_permlane32_swap(__float_as_uint(a), __float_as_uint(b), false, false);
  return f32x2{__uint_as_float(s[0]), __uint_as_float(s[1])};
}
__device__ __forceinline__ float xrows_sum(float x) {
  f32x2 s = swap16(x, x); x = s[0] + s[1];
  f32x2 t = swap32(x, x); return t[0] + t[1];
}
__device__ __forceinline__ float wave_sum(float x) {
  x += dpp<XOR1>(x); x += dpp<XOR2>(x); x += dpp<ROR4>(x); x += dpp<ROR8>(x);
  return xrows_sum(x);
}
typedef __amdgpu_buffer_rsrc_t rsrc_t;
__device__ __forceinline__ rsrc_t mk_rsrc(const void* p, u32 bytes) { return __builtin_amdgcn_make_buffer_rsrc((void*)p, 0, (int)bytes, 0x00020000); }
__device__ __forceinline__ u32x4 ldw(rsrc_t r, u32 voff, u32 soff) { return __builtin_amdgcn_raw_buffer_load_b128(r, voff, soff, WAUX); }
#define LGKM0() asm volatile("s_waitcnt lgkmcnt(0)" ::: "memory")
__device__ __forceinline__ void wg_barrier() {
  LGKM0();
  __builtin_amdgcn_s_barrier();
  asm volatile("" ::: "memory");
}
__device__ __forceinline__ void st_gran(GAS u64* g, u32 tag, u32 v) { __hip_atomic_store(g, ((u64)tag << 32) | (u64)v, RLX_AGENT); }
__device__ __forceinline__ u64 ld_gran(const GAS u64* g) { return __hip_atomic_load(g, RLX_AGENT); }
#define PSLEEP 1
__device__ __forceinline__ void lds_wait_ge(volatile LAS u32* p, u32 need) {
  for (unsigned spins = 0; (u32)__builtin_amdgcn_readfirstlane(*p) < need && spins < (SPIN_CAP << 4); ++spins) __builtin_amdgcn_s_sleep(PSLEEP);
  asm volatile("" ::: "memory");
}
#define PIN8(a) asm volatile("" : "+v"(a[0]), "+v"(a[1]), "+v"(a[2]), "+v"(a[3]), "+v"(a[4]), "+v"(a[5]), "+v"(a[6]), "+v"(a[7]))
#define PIN4(a) asm volatile("" : "+v"(a[0]), "+v"(a[1]), "+v"(a[2]), "+v"(a[3]) : : "memory")

#define STAMP(i) do { } while (0)

__device__ __forceinline__ float k_unit(const u32x4 (&k)[4], const u32x4& q0, const u32x4& q1) {
  float e[4];
#pragma unroll
  for (int i = 0; i < 4; ++i) {
    float a = 0.f, b = 0.f;
#pragma unroll
    for (int j = 0; j < 4; ++j) { a = dot2(k[i][j], q0[j], a); b = dot2(k[i][j], q1[j], b); }
    const f32x2 s = swap16(a, b);
    e[i] = s[0] + s[1];
  }
  const float f0 = dppm<ROR8, 0x3>(e[2], e[0]) + dppm<ROR8, 0xc>(e[0], e[2]);
  const float f1 = dppm<ROR8, 0x3>(e[3], e[1]) + dppm<ROR8, 0xc>(e[1], e[3]);
  float g = dppm<SHL4, 0x5>(f1, f0) + dppm<SHR4, 0xa>(f0, f1);
  g += dpp<XOR1>(g); g += dpp<XOR2>(g);
  return rbf(g);
}
__device__ __forceinline__ void v_piece(f32x2 (&acc)[8], float p0, float p1, u32x4 v) {
#pragma unroll
  for (int j = 0; j < 4; ++j) {
    const f32x2 vv = {__uint_as_float(v[j] << 16), __uint_as_float(v[j] & 0xffff0000u)};
    acc[j] = __builtin_elementwise_fma(vv, f32x2{p0, p0}, acc[j]);
    acc[4 + j] = __builtin_elementwise_fma(vv, f32x2{p1, p1}, acc[4 + j]);
  }
}

#define DMA_W(R, VO, SO, M0) asm volatile("s_mov_b32 m0, %0\n\ts_nop 0\n\tbuffer_load_dwordx4 %1, %2, %3 offen nt lds" :: "s"(M0), "v"(VO), "s"(R), "s"(SO) : "m0", "memory")
#define VM0() asm volatile("s_waitcnt vmcnt(0)" ::: "memory")
__device__ __forceinline__ s16x8 tr_frag(const LAS unsigned char* p, int four_rows) {
  const s16x4 b0 = __builtin_amdgcn_ds_read_tr16_b64_v4i16((LAS s16x4*)p);
  const s16x4 b1 = __builtin_amdgcn_ds_read_tr16_b64_v4i16((LAS s16x4*)(p + four_rows));
  return s16x8{b0[0], b0[1], b0[2], b0[3], b1[0], b1[1], b1[2], b1[3]};
}
__device__ __forceinline__ f32x4 mfma16(s16x8 a, s16x8 b, f32x4 c) {
  return __builtin_amdgcn_mfma_f32_16x16x32_bf16(__builtin_bit_cast(bf16x8, a), __builtin_bit_cast(bf16x8, b), c, 0, 0, 0);
}
template <int KS> __device__ __forceinline__ f32x4 gemm8x16(const LAS unsigned char* A, int astride, const LAS unsigned char* Bt, int bstride, int lane) {
  f32x4 acc = {0.f, 0.f, 0.f, 0.f};
  const int g = lane >> 4;
  const LAS unsigned char* ap = A + (lane & 7) * astride + 16 * g;
  const LAS unsigned char* bp = Bt + (8 * g + ((lane & 15) >> 2)) * bstride + 8 * (lane & 3);
#pragma unroll
  for (int ks = 0; ks < KS; ++ks) acc = mfma16(*(const LAS s16x8*)(ap + 64 * ks), tr_frag(bp + ks * 32 * bstride, 4 * bstride), acc);
  return acc;
}
__device__ __forceinline__ u32 pair_of(f32x4 acc, int lane, int& batch) {
  const int g = lane >> 4;
  const float lo = (g >> 1) ? acc[2] : acc[0], hi = (g >> 1) ? acc[3] : acc[1];
  const float send = (lane & 1) ? lo : hi, keep = (lane & 1) ? hi : lo;
  const float got = dpp<XOR1>(send);
  batch = 4 * (g & 1) + (g >> 1) * 2 + (lane & 1);
  return (lane & 1) ? pk2(got, keep) : pk2(keep, got);
}
__device__ __forceinline__ void st_gran2(rsrc_t wsr, u32 voff, u32 soff, u32 tag, float a, float b) {
  __builtin_amdgcn_raw_buffer_store_b128(u32x4{__float_as_uint(a), tag, __float_as_uint(b), tag}, wsr, voff + soff, 0, 16);
}
__device__ __forceinline__ void pub_tile(rsrc_t wsr, u32 soff, u32 pstride, f32x4 acc, u32 tag, int lane) {
  const int g = lane >> 4;
  st_gran2(wsr, (u32)(2 * (g & 1) + (g >> 1)) * pstride + (lane & 15) * 16, soff, tag, (g >> 1) ? acc[2] : acc[0], (g >> 1) ? acc[3] : acc[1]);
}
__device__ __forceinline__ void nap(int units) {
  for (int i = 0; i < units; ++i) __builtin_amdgcn_s_sleep(127);
}
template <int N, int SLEEP> __device__ __forceinline__ void sweep1(rsrc_t wsr, u32 voff, u32 soff, u32 stride, u32 tag, u32 (&v)[N]) {
  unsigned have = 0;
#pragma unroll
  for (int i = 0; i < N; ++i) v[i] = 0u;
  for (unsigned spins = 0;; ++spins) {
    u32x2 t[N];
    asm volatile("" ::: "memory");
#pragma unroll
    for (int i = 0; i < N; ++i) t[i] = __builtin_amdgcn_raw_buffer_load_b64(wsr, voff, ((have >> i) & 1u) ? 0x40000000u : soff + i * stride, 16);
#pragma unroll
    for (int i = 0; i < N; ++i) {
      v[i] = ((have >> i) & 1u) ? v[i] : t[i][0];
      have |= __all(t[i][1] == tag) ? 1u << i : 0u;
    }
    if (have == (1u << N) - 1u || spins > SPIN_CAP) break;
    __builtin_amdgcn_s_sleep(SLEEP);
  }
  asm volatile("" ::: "memory");
}
template <int N, int SLEEP> __device__ __forceinline__ void sweep2(rsrc_t wsr, u32 voff, u32 soff, u32 stride, u32 tag, f32x2 (&v)[N]) {
  unsigned have = 0;
#pragma unroll
  for (int i = 0; i < N; ++i) v[i] = f32x2{0.f, 0.f};
  for (unsigned spins = 0;; ++spins) {
    u32x4 t[N];
    asm volatile("" ::: "memory");
#pragma unroll
    for (int i = 0; i < N; ++i) t[i] = __builtin_amdgcn_raw_buffer_load_b128(wsr, voff, ((have >> i) & 1u) ? 0x40000000u : soff + i * stride, 16);
#pragma unroll
    for (int i = 0; i < N; ++i) {
      v[i] = ((have >> i) & 1u) ? v[i] : f32x2{__uint_as_float(t[i][0]), __uint_as_float(t[i][2])};
      have |= __all(t[i][1] == tag && t[i][3] == tag) ? 1u << i : 0u;
    }
    if (have == (1u << N) - 1u || spins > SPIN_CAP) break;
    __builtin_amdgcn_s_sleep(SLEEP);
  }
  asm volatile("" ::: "memory");
}

__global__ void __launch_bounds__(NTHR, 3)
fused_decode(const u16* __restrict__ x_, const u16* __restrict__ kc_, const u16* __restrict__ vc_, const u16* __restrict__ wq_,
             const u16* __restrict__ wo_, const u16* __restrict__ w1_, const u16* __restrict__ w2_, u16* __restrict__ out_,
             unsigned char* ws_) {
  extern __shared__ __attribute__((aligned(16))) unsigned char smem[];
  LAS unsigned char* L = (LAS unsigned char*)smem;
  GAS unsigned char* ws = (GAS unsigned char*)ws_;
  const int tid = threadIdx.x, lane = tid & 63, wave = __builtin_amdgcn_readfirstlane(tid >> 6);
  const int bid = blockIdx.x, kvh = bid & 7, jj = bid >> 3;
  const u32 ldsb = (u32)(uintptr_t)L;
  const rsrc_t wsr = mk_rsrc(ws_, WS_ST);
  const int c = wave - 2;

  if (jj >= 24) {
    const int t = jj - 24;
    const bool mid = wave >= 2 && wave <= 9, ffn = t < 7 && !(SKIP & 1);
    if (wave == 0) STAMP(0);
    if (mid) {
      u32x4 xw;
      asm volatile("global_load_dwordx4 %0, %1, off" : "=v"(xw) : "v"(x_ + c * D + 8 * (lane < 56 ? lane : 55)) : "memory");
      {
        const rsrc_t r = mk_rsrc(wq_, (SKIP & 8) ? 0u : (u32)D * HQ * DH * 2);
        const u32 vo = (lane >> 3) * (HQ * DH * 2) + (lane & 7) * 16;
#pragma unroll
        for (int it = 0; it < 7; ++it) DMA_W(r, vo, (56 * c + 8 * it) * (HQ * DH * 2) + kvh * 1024 + t * 128, ldsb + S_WQ + (7 * c + it) * 1024);
      }
      asm volatile("s_waitcnt vmcnt(7)" : "+v"(xw) :: "memory");
      float xv[8];
#pragma unroll
      for (int j = 0; j < 4; ++j) { xv[2 * j] = __uint_as_float(xw[j] << 16); xv[2 * j + 1] = __uint_as_float(xw[j] & 0xffff0000u); }
      float s = ((xv[0] + xv[1]) + (xv[2] + xv[3])) + ((xv[4] + xv[5]) + (xv[6] + xv[7]));
      const float mean = wave_sum(lane < 56 ? s : 0.f) * (1.f / D);
      float ss = 0.f;
#pragma unroll
      for (int j = 0; j < 8; ++j) { const float d = xv[j] - mean; ss = fmaf(d, d, ss); }
      const float var = rbf(wave_sum(lane < 56 ? ss : 0.f) * (1.f / D));
      const float mb = rbf(mean), sd = rbf(sqrtf(rbf(var + 1e-5f)));
      u32x4 o;
#pragma unroll
      for (int j = 0; j < 4; ++j) o[j] = pk2(rbf(xv[2 * j] - mb) / sd, rbf(xv[2 * j + 1] - mb) / sd);
      if (lane < 56) *(LAS u32x4*)(L + S_XN + c * XN_STRIDE + 16 * lane) = o;
      VM0();
    } else if (wave == 10) {
      const u32 e = __hip_atomic_load((const GAS u32*)(ws + WS_EPOCH), RLX_AGENT);
      if (lane == 0) *(LAS u32*)(L + S_TAG) = e + 1u;
    }
    wg_barrier();
    const u32 tag = __builtin_amdgcn_readfirstlane(*(const LAS u32*)(L + S_TAG));
    if (wave >= 2 && wave <= 5) {
      const f32x4 acc = gemm8x16<14>(L + S_XN, XN_STRIDE, L + S_WQ + 32 * c, 128, lane);
      int bb; const u32 v = pair_of(acc, lane, bb);
      st_gran((GAS u64*)(ws + WS_H0) + (size_t)(bb * 8 + kvh) * 256 + 32 * t + 8 * c + ((lane & 15) >> 1), tag, v);
      if (wave == 2) STAMP(9);
    } else if (ffn) {
      const int w = wave < 2 ? wave : wave - 4;
      const rsrc_t r1 = mk_rsrc(w1_, (SKIP & 32) ? 0u : (u32)D * DFF * 2), r2 = mk_rsrc(w2_, (SKIP & 64) ? 0u : (u32)D * DFF * 2);
      const u32 vo1 = (lane >> 2) * (DFF * 2) + (lane & 3) * 16, vo2 = (lane >> 3) * (D * 2) + (lane & 7) * 16;
#pragma unroll
      for (int it = 0; it < 4; ++it) DMA_W(r1, vo1, (4 * w + it) * 16 * (DFF * 2) + kvh * 448 + t * 64, ldsb + S_W1 + (4 * w + it) * 1024);
#pragma unroll
      for (int it = 0; it < 4; ++it) DMA_W(r2, vo2, (kvh * 224 + (4 * w + it) * 8) * (D * 2) + t * 128, ldsb + S_W2 + (4 * w + it) * 1024);
      VM0();
    }
    wg_barrier();
    if (mid) {
      const rsrc_t r = mk_rsrc(wo_, (SKIP & 4) ? 0u : (u32)D * HQ * DH * 2);
#pragma unroll
      for (int it = 0; it < 7; ++it) {
        const int i = lane + 64 * (7 * c + it), row = i / 14;
        DMA_W(r, (u32)(row * (D * 2) + (i - row * 14) * 16), (2 * kvh + (t >> 2)) * 256 * (D * 2) + (t & 3) * 224, ldsb + S_WQ + (7 * c + it) * 1024);
      }
      VM0();
    } else if (ffn && !(SKIP & 128)) {
      if (wave < 2) {
        f32x4 acc = gemm8x16<14>(L + S_XN, XN_STRIDE, L + S_W1 + 32 * wave, 64, lane);
#pragma unroll
        for (int i = 0; i < 4; ++i) acc[i] = fmaxf(acc[i], 0.f);
        int bb; const u32 v = pair_of(acc, lane, bb);
        st_gran((GAS u64*)(ws + WS_HH) + (size_t)(kvh * 8 + bb) * 112 + 16 * t + 8 * wave + ((lane & 15) >> 1), tag, v);
      } else {
        __builtin_amdgcn_s_sleep(100);
        f32x2 v[7];
        sweep2<7, 40>(wsr, lane * 16, WS_HH + kvh * 7168, 1024, tag, v);
#pragma unroll
        for (int i = 0; i < 7; ++i) { const int idx = lane + 64 * i, bb = idx / 56; *(LAS f32x2*)(L + S_HA + bb * HA_STRIDE + 8 * (idx - bb * 56)) = v[i]; }
      }
    }
    wg_barrier();
    if (wave == 10) return;
    if (mid) {
      if (ffn && c < 4 && !(SKIP & 128)) {
        const f32x4 acc = gemm8x16<7>(L + S_HA, HA_STRIDE, L + S_W2 + 32 * c, 128, lane);
        pub_tile(wsr, WS_HF + ((4 * t + c) * 8 + kvh) * 256, 28 * 8 * 256, acc, tag, lane);
      }
      s16x8 wb[8];
      {
        const LAS unsigned char* bp = L + S_WQ + (8 * (lane >> 4) + ((lane & 15) >> 2)) * 224 + 32 * (c < 7 ? c : 6) + 8 * (lane & 3);
#pragma unroll
        for (int ks = 0; ks < 8; ++ks) wb[ks] = tr_frag(bp + ks * 32 * 224, 4 * 224);
        PIN8(wb);
      }
      float w0, w1, w2;
      {
        nap(4);
        u32 v[1];
        sweep1<1, 100>(wsr, (u32)((lane & 15) < 12 ? (lane & 15) : 0) * 8, WS_ML + (c * 8 + kvh) * 128, 0, tag, v);
        const int h = t >> 2;
        float mv[3], lv[3];
#pragma unroll
        for (int s = 0; s < 3; ++s) {
          mv[s] = __uint_as_float(__builtin_amdgcn_readlane(v[0], 4 * s + h));
          lv[s] = __uint_as_float(__builtin_amdgcn_readlane(v[0], 4 * s + 2 + h));
        }
        const float M = fmaxf(fmaxf(mv[0], mv[1]), mv[2]);
        w0 = __builtin_amdgcn_exp2f((mv[0] - M) * CEXP); w1 = __builtin_amdgcn_exp2f((mv[1] - M) * CEXP); w2 = __builtin_amdgcn_exp2f((mv[2] - M) * CEXP);
        const float Ls = fmaf(w0, lv[0], fmaf(w1, lv[1], w2 * lv[2]));
        w0 /= Ls; w1 /= Ls; w2 /= Ls;
      }
      if (c == 0) STAMP(10);
      nap(2);
      {
        u32 hv[1];
        sweep1<1, 1>(wsr, (u32)((lane & 3) < 3 ? (lane & 3) : 0) * 8, WS_N1 + (kvh * 8 + t) * 256 + c * 32, 0, tag, hv);
        if (c == 7) STAMP(24);
        f32x2 v[6];
        sweep2<6, 1>(wsr, lane * 16, WS_H1 + ((2 * kvh + (t >> 2)) * 8 + c) * 6144, 1024, tag, v);
#pragma unroll
        for (int i = 0; i < 2; ++i)
          *(LAS u32*)(L + S_OA + c * OA_STRIDE + 256 * i + 4 * lane) =
              pk2(fmaf(w0, v[i][0], fmaf(w1, v[2 + i][0], w2 * v[4 + i][0])), fmaf(w0, v[i][1], fmaf(w1, v[2 + i][1], w2 * v[4 + i][1])));
      }
      if (c == 7) STAMP(6);
      wg_barrier();
      if (c < 7) {
        const LAS unsigned char* ap = L + S_OA + (lane & 7) * OA_STRIDE + 16 * (lane >> 4);
        f32x4 s0 = {0.f, 0.f, 0.f, 0.f}, s1 = {0.f, 0.f, 0.f, 0.f};
#pragma unroll
        for (int ks = 0; ks < 8; ks += 2) {
          s0 = mfma16(*(const LAS s16x8*)(ap + 64 * ks), wb[ks], s0);
          s1 = mfma16(*(const LAS s16x8*)(ap + 64 * ks + 64), wb[ks + 1], s1);
        }
        pub_tile(wsr, WS_H2 + ((7 * (t & 3) + c) * 16 + 2 * kvh + (t >> 2)) * 256, 28 * 16 * 256, s0 + s1, tag, lane);
        if (c == 0) STAMP(21);
      } else if (lane < 28) {
        st_gran((GAS u64*)(ws + WS_N2) + ((lane / 7) * 28 + 7 * (t & 3) + lane % 7) * 16 + 2 * kvh + (t >> 2), tag, 1u);
      }
      return;
    }
    if (t == 7) return;
    const int f = 2 * (7 * kvh + t) + wave, bp = f / 28, ct = f - bp * 28;
    f32x2 gate, xres;
    {
      const GAS u16* xp = (const GAS u16*)x_ + 2 * bp * D + 16 * ct + (lane & 15);
      xres = f32x2{bf2f(xp[0]), bf2f(xp[D])};
      nap(1);
      f32x2 v[2];
      if (SKIP & 129) { v[0] = v[1] = xres; } else
      sweep2<2, 100>(wsr, lane * 16, WS_HF + f * 2048, 1024, tag, v);
      const float g0 = rbf(xrows_sum(v[0][0] + v[1][0])), g1 = rbf(xrows_sum(v[0][1] + v[1][1]));
      gate = f32x2{rbf(g0 / (1.f + __expf(-g0))), rbf(g1 / (1.f + __expf(-g1)))};
    }
    const int odd = (lane >> 4) & 1;
    const float gsel = odd ? gate[1] : gate[0], xsel = odd ? xres[1] : xres[0];
    if (wave == 0) STAMP(5);
    wg_barrier();
    if (wave == 0) STAMP(7);
    {
      u32 hv[1];
      sweep1<1, 1>(wsr, (lane & 15) * 8, WS_N2 + f * 128, 0, tag, hv);
      if (wave == 0) STAMP(22);
      f32x2 v[4];
      sweep2<4, 1>(wsr, lane * 16, WS_H2 + f * 4096, 1024, tag, v);
      if (wave == 0) STAMP(23);
      const f32x2 s = (v[0] + v[1]) + (v[2] + v[3]);
      const f32x2 u = swap16(s[0], s[1]);
      const float h = u[0] + u[1];
      const f32x2 w = swap32(h, h);
      const float sv = rbf(w[0] + w[1]);
      const float r = xsel + rbf(sv * gsel);
      if (lane < 32) out_[(2 * bp + odd) * D + 16 * ct + (lane & 15)] = (u16)f2bf(r);
      if (f == 0 && lane == 0) __hip_atomic_store((GAS u32*)(ws + WS_EPOCH), tag, RLX_AGENT);
    }
    if (wave == 0) STAMP(8);
    return;
  }

  volatile LAS u32* ctl = (volatile LAS u32*)(L + L_CTL);
  const int b = jj / 3, sp = jj - 3 * b, pair = b * 8 + kvh;
  const int nu = sp == 2 ? 170 : 171;

  if (wave < 2) {
    __builtin_amdgcn_s_setprio(3);
    if (wave == 0) STAMP(0);
    const rsrc_t kr = mk_rsrc(kc_, (u32)B * HKV * SK * DH * 2), vr = mk_rsrc(vc_, (u32)B * HKV * SK * DH * 2);
    const u32 so0 = (u32)pair * (SK * DH * 2) + (u32)sp * (171 * 4096) + wave * 4096, vo = lane * 16, lb = ldsb + wave * 4096;
    constexpr int WU = WIN / 4;
#define DMA_UNIT(R, SO, M0, PUB) asm volatile("ds_read_b32 %0, %8\n\ts_mov_b32 m0, %1\n\t" \
      "s_waitcnt vmcnt(%5)\n\tbuffer_load_dwordx4 %2, %3, %4 offen " KVPOL " lds\n\t" \
      "s_waitcnt vmcnt(%5)\n\tbuffer_load_dwordx4 %2, %3, %4 offen offset:1024 " KVPOL " lds\n\t" \
      "s_waitcnt vmcnt(%5)\n\tbuffer_load_dwordx4 %2, %3, %4 offen offset:2048 " KVPOL " lds\n\t" \
      "s_waitcnt vmcnt(%5)\n\tbuffer_load_dwordx4 %2, %3, %4 offen offset:3072 " KVPOL " lds\n\t" \
      "s_waitcnt lgkmcnt(0)\n\tds_write_b32 %6, %7" \
      : "=&v"(dn) : "s"(M0), "v"(vo), "s"(R), "s"(SO), "n"(WIN - 1), "v"(pubaddr), "v"(PUB), "v"(dnaddr) : "m0", "memory")
    u32 dn;
    const u32 dnaddr = ldsb + L_CTL + 32 + 8 * (lane & 3) + 4 * wave;
    {
      const u32 pubaddr = ldsb + L_CTL + 28;
#pragma unroll
      for (int u = 0; u < WU; ++u) DMA_UNIT(kr, so0 + u * 8192, lb + u * 8192, 0u);
    }
    wg_barrier();
    const u32 pubaddr = ldsb + L_CTL + 4 * wave;
    dn = 0u;
#pragma unroll
    for (int u = WU; u < 8; ++u) DMA_UNIT(kr, so0 + u * 8192, lb + u * 8192, (u32)(u - WU + 1));
    u32 nfull = 0; const u64 lrt0 = __builtin_amdgcn_s_memrealtime();
    u32 pub = 0, iss = 8;
    const int rem = (nu - 8 * (NGRP - 1) + 1 - wave) >> 1;
#define STEP(j) do { \
        if ((int)__builtin_amdgcn_readlane(dn, j) < need) {     \
          ++nfull; VM0(); pub = iss; ctl[wave] = pub; lds_wait_ge(ctl + 8 + 2 * (j) + wave, (u32)need); } \
        const u32 pv = iss - WU + 1; pub = pub > pv ? pub : pv; \
        DMA_UNIT(rr, so + (j) * 8192, m0 + (j) * 8192, pub); ++iss; } while (0)
    for (int g = 2; g < 2 * NGRP; ++g) {
      const int gk = g < NGRP ? g : g - NGRP;
      const int need = g - NSLOT / 8 + 1;
      const u32 so = so0 + (u32)gk * 32768, m0 = lb + (u32)(g & (NSLOT / 8 - 1)) * 32768;
      const rsrc_t rr = g < NGRP ? kr : vr;
      STEP(0);
      if (gk == NGRP - 1 && rem < 2) continue;
      STEP(1);
      if (gk == NGRP - 1) continue;
      STEP(2);
      STEP(3);
    }
#pragma unroll
    for (int r = WU - 1; r >= 0; --r) {
      asm volatile("s_waitcnt vmcnt(%0)" :: "n"(4 * r) : "memory");
      pub = pub > iss - r ? pub : iss - r;
      ctl[wave] = pub;
    }
    if (wave == 0) STAMP(2);
    return;
  }

  if (wave == NC + 2) {
    const u64 rt0 = __builtin_amdgcn_s_memrealtime();
    u32 tag = __hip_atomic_load((const GAS u32*)(ws + WS_EPOCH), RLX_AGENT);
    if (lane < 16) ctl[lane] = 0u;
    wg_barrier();
    asm volatile("" : "+v"(tag));
    tag = __builtin_amdgcn_readfirstlane(tag) + 1u;
    ctl[3] = tag;
    while (__builtin_amdgcn_s_memrealtime() - rt0 < H0_AT) __builtin_amdgcn_s_sleep(4);
    f32x2 v[2];
    if (SKIP & 16) { v[0] = v[1] = f32x2{1e-3f, 1e-3f}; } else
    sweep2<2, 16>(wsr, lane * 16, WS_H0 + pair * 2048, 1024, tag, v);
    *(LAS f32x2*)(L + L_Q + 8 * lane) = v[0];
    *(LAS f32x2*)(L + L_Q + 512 + 8 * lane) = v[1];
    LGKM0();
    ctl[2] = 1u;
    STAMP(1);
    return;
  }

  const int cl = lane & 31;
  wg_barrier();
  const LAS unsigned char* rp = L + L_RING + c * 4096 + lane * 16;
  volatile LAS u32* landed = ctl + (c & 1);
  const int n0 = (c >> 1) + 1, nv = n0 + ((nu + 1 - (c & 1)) >> 1);
#define TAKE(dst, g, need) do { lds_wait_ge(landed, (u32)(need)); \
    _Pragma("unroll") for (int i_ = 0; i_ < 4; ++i_) dst[i_] = *(const LAS u32x4*)(rp + ((g) & 3) * 32768 + i_ * 1024); \
    LGKM0(); ctl[8 + c] = (u32)((g) + 1); PIN4(dst); } while (0)
  LAS float* scp = (LAS float*)(L + L_SC + c * 1408);
  LAS float* scw = scp + (lane >> 5) * 8 + ((lane >> 2) & 3) * 2 + ((lane >> 4) & 1);
  const int ng = 8 * (NGRP - 1) + c < nu ? NGRP : NGRP - 1;
  float m = -INFINITY;
  {
    u32x4 k0[4], k1[4], k2[4], k3[4], k4[4], k5[4], k6[4], k7[4];
    int gt = 0;
#define PRE(kk, i) if (gt == (i) && __builtin_amdgcn_readfirstlane(ctl[2]) == 0u) { TAKE(kk, i, n0 + 4 * (i)); gt = (i) + 1; }
    PRE(k0, 0) PRE(k1, 1) PRE(k2, 2) PRE(k3, 3) PRE(k4, 4) PRE(k5, 5) PRE(k6, 6) PRE(k7, 7)
    lds_wait_ge(ctl + 2, 1u);
    if (c == 3) STAMP(25);
    const u32x4 q0 = *(const LAS u32x4*)(L + L_Q + 16 * cl), q1 = *(const LAS u32x4*)(L + L_Q + 512 + 16 * cl);
    scw[16 * (NGRP - 1)] = -INFINITY;
    int gp = 0;
#pragma unroll 1
    while (gp < ng) {
      if (gt == gp) { TAKE(k0, gt, n0 + 4 * gt); ++gt; }
      const int n = gt - gp, base = gt;
#define POST(kk, i) if (n > (i)) { const float s_ = k_unit(kk, q0, q1); scw[16 * (gp + (i))] = s_; m = fmaxf(m, s_); \
        asm volatile("" : "+v"(m)); \
        if (gt == base + (i) && gt < ng && (int)__builtin_amdgcn_readfirstlane(*landed) >= n0 + 4 * gt) { TAKE(kk, gt, 0); ++gt; } }
      POST(k0, 0) POST(k1, 1) POST(k2, 2) POST(k3, 3) POST(k4, 4) POST(k5, 5) POST(k6, 6) POST(k7, 7)
      gp = base;
    }
  }
  ctl[8 + c] = (u32)NGRP;
  if (c == 2) STAMP(27);
  const u32 tag = __builtin_amdgcn_readfirstlane(ctl[3]);
  {
    m = fmaxf(m, dpp<ROR4>(m)); m = fmaxf(m, dpp<ROR8>(m));
    { const f32x2 t = swap32(m, m); m = fmaxf(t[0], t[1]); }
    if ((lane & 47) == 0) ((LAS float*)(L + L_MX))[2 * c + (lane >> 4)] = m;
    LGKM0();
    if (lane == 0) __hip_atomic_fetch_add((LAS u32*)(L + L_CTL + 20), 1u, __ATOMIC_RELAXED, __HIP_MEMORY_SCOPE_WORKGROUP);
    lds_wait_ge(ctl + 5, 8u);
    float M = ((const LAS float*)(L + L_MX))[lane & 15];
    M = fmaxf(M, dpp<XOR2>(M)); M = fmaxf(M, dpp<ROR4>(M)); M = fmaxf(M, dpp<ROR8>(M));
    const float M0 = dpp<0xA0>(M), M1 = dpp<0xF5>(M);
    float l0 = 0.f, l1 = 0.f;
#pragma unroll
    for (int i = 0; i < 2; ++i) {
      if (i == 0 || lane < 24) {
        f32x4 s = *(const LAS f32x4*)(scp + 4 * (lane + 64 * i));
        s[0] = __builtin_amdgcn_exp2f((s[0] - M0) * CEXP); s[1] = __builtin_amdgcn_exp2f((s[1] - M1) * CEXP);
        s[2] = __builtin_amdgcn_exp2f((s[2] - M0) * CEXP); s[3] = __builtin_amdgcn_exp2f((s[3] - M1) * CEXP);
        *(LAS f32x4*)(scp + 4 * (lane + 64 * i)) = s;
        l0 += s[0] + s[2]; l1 += s[1] + s[3];
      }
    }
    l0 = wave_sum(l0); l1 = wave_sum(l1);
    if (lane == 0) *(LAS f32x2*)(L + L_MX + 64 + 8 * c) = f32x2{l0, l1};
    LGKM0();
    u32 old = 0;
    if (lane == 0) old = __hip_atomic_fetch_add((LAS u32*)(L + L_CTL + 16), 1u, __ATOMIC_RELAXED, __HIP_MEMORY_SCOPE_WORKGROUP);
    old = __builtin_amdgcn_readfirstlane(old);
    asm volatile("" ::: "memory");
    if (old == 7u) {
      float Ls = ((const LAS float*)(L + L_MX + 64))[lane & 15];
      Ls += dpp<XOR2>(Ls); Ls += dpp<ROR4>(Ls); Ls += dpp<ROR8>(Ls);
      if (lane < 4) st_gran((GAS u64*)(ws + WS_ML) + pair * 16 + sp * 4 + lane, tag, __float_as_uint((lane & 2) ? Ls : M));
    }
  }
  if (c == 2) STAMP(28);
  f32x2 acc[8];
#pragma unroll
  for (int j = 0; j < 8; ++j) acc[j] = f32x2{0.f, 0.f};
  const LAS float* scr = scp + (lane >> 5) * 8;
  GAS u64* notice = (GAS u64*)(ws + WS_N1) + (kvh * 8 + (lane & 7)) * 32 + b * 4 + sp;
  const bool lastw = c == ((nu - 1) & 7);
#pragma unroll 1
  for (int g = 0; g < ng; ++g) {
    const f32x4 P0 = *(const LAS f32x4*)(scr + 16 * g), P1 = *(const LAS f32x4*)(scr + 16 * g + 4);
    u32x4 v[4];
    TAKE(v, NGRP + g, nv + 4 * g);
    if (HINT_POS == 0 && lastw && g == NGRP - 1 && lane < 8) st_gran(notice, tag, 1u);
    v_piece(acc, P0[0], P0[1], v[0]); v_piece(acc, P0[2], P0[3], v[1]);
    v_piece(acc, P1[0], P1[1], v[2]); v_piece(acc, P1[2], P1[3], v[3]);
  }
  if (HINT_POS == 1 && lastw && lane < 8) st_gran(notice, tag, 1u);
  if (c == 1) STAMP(3);
  {
    float o[8];
#pragma unroll
    for (int j = 0; j < 8; ++j) { const f32x2 t = swap32(acc[j >> 1][j & 1], acc[4 + (j >> 1)][j & 1]); o[j] = t[0] + t[1]; }
    LAS float* dst = (LAS float*)(L + L_RING + c * 4096) + (lane >> 5) * 256 + 8 * cl;
    *(LAS f32x4*)dst = f32x4{o[0], o[1], o[2], o[3]};
    *(LAS f32x4*)(dst + 4) = f32x4{o[4], o[5], o[6], o[7]};
  }
  wg_barrier();
  if (c < 4) {
    const LAS f32x2* pp = (const LAS f32x2*)(L + L_RING) + c * 64 + lane;
    const f32x2 o = ((pp[0] + pp[512]) + (pp[1024] + pp[1536])) + ((pp[2048] + pp[2560]) + (pp[3072] + pp[3584]));
    st_gran2(wsr, lane * 16, WS_H1 + (((2 * kvh + (c >> 1)) * 8 + b) * 3 + sp) * 2048 + (c & 1) * 1024, tag, o[0], o[1]);
  } else if (HINT_POS == 2 && c == 4 && lane < 8) st_gran(notice, tag, 1u);
  if (c == 1) STAMP(4);
}

extern "C" void kernel_launch(void* const* d_in, const int* in_sizes, int n_in, void* d_out, int out_size, void* d_ws,
                              size_t ws_size, hipStream_t stream) {
  (void)hipFuncSetAttribute((const void*)fused_decode, hipFuncAttributeMaxDynamicSharedMemorySize, L_BYTES);
  fused_decode<<<256, NTHR, L_BYTES, stream>>>((const u16*)d_in[0], (const u16*)d_in[1], (const u16*)d_in[2], (const u16*)d_in[3],
                                               (const u16*)d_in[4], (const u16*)d_in[6], (const u16*)d_in[7], (u16*)d_out,
                                               (unsigned char*)d_ws);
}
```

```cpp
#include <hip/hip_runtime.h>
#include <hip/hip_bf16.h>
#include <stdint.h>

typedef unsigned u32;
typedef unsigned long long u64;
typedef unsigned short u16;
typedef unsigned u32x4 __attribute__((ext_vector_type(4)));
typedef unsigned u32x2 __attribute__((ext_vector_type(2)));
typedef float f32x4 __attribute__((ext_vector_type(4)));
typedef float f32x2 __attribute__((ext_vector_type(2)));
typedef short s16x4 __attribute__((ext_vector_type(4)));
typedef short s16x8 __attribute__((ext_vector_type(8)));
typedef __attribute__((ext_vector_type(2))) __bf16 bf16x2;
typedef __attribute__((ext_vector_type(8))) __bf16 bf16x8;
#define GAS __attribute__((address_space(1)))
#define LAS __attribute__((address_space(3)))
#define RLX_AGENT __ATOMIC_RELAXED, __HIP_MEMORY_SCOPE_AGENT

constexpr int B = 8, D = 448, HQ = 16, HKV = 8, SK = 4096, DH = 256, DFF = 1792;
constexpr int NC = 8, NTHR = (NC + 3) * 64;
constexpr int NSLOT = 32, NGRP = 22;
constexpr float CEXP = 0.0625f * 1.4426950408889634f;
constexpr unsigned SPIN_CAP = 1u << 18;

#define TAU 337
#define PACE_A (-50)
constexpr int WIN = 20, WU = (WIN + 3) / 4;
constexpr int WA = 10, LAG1 = 15, LAG2 = 45, LAG3 = 90;
constexpr unsigned H0_AT = 600;
constexpr unsigned T_W = 450, W_GAP = 190;
constexpr int D2 = 20;

constexpr u32 WS_EPOCH = 0;
constexpr u32 WS_H0 = 65536;
constexpr u32 WS_ML = WS_H0 + 64 * 256 * 8;
constexpr u32 WS_N1 = WS_ML + 64 * 16 * 8;
constexpr u32 WS_N2 = WS_N1 + 64 * 32 * 8;
constexpr u32 WS_HH = WS_N2 + 112 * 16 * 8;
constexpr u32 WS_HF = WS_HH + 8 * 896 * 8;
constexpr u32 WS_H1 = WS_HF + 112 * 8 * 32 * 4;
constexpr u32 WS_H2 = WS_H1 + 16 * 8 * 3 * 256 * 4;
constexpr u32 WS_END = WS_H2 + 112 * 16 * 32 * 4;

constexpr int L_RING = 0;
constexpr int L_Q = NSLOT * 4096;
constexpr int L_SC = L_Q + 1024;
constexpr int L_MX = L_SC + 8 * 1408;
constexpr int L_CTL = L_MX + 192;
constexpr int L_BYTES = L_CTL + 64;
constexpr int XN_STRIDE = 912, HA_STRIDE = 464, OA_STRIDE = 528;
constexpr int S_WQ = 0;
constexpr int S_W1 = S_WQ + 57344;
constexpr int S_W2 = S_W1 + 28672;
constexpr int S_XN = S_W2 + 28672;
constexpr int S_HA = S_XN + 8 * XN_STRIDE;
constexpr int S_OA = S_HA + 8 * HA_STRIDE;
constexpr int S_TAG = S_OA + 8 * OA_STRIDE;
static_assert(L_BYTES <= 160 * 1024 && L_BYTES > 80 * 1024 && S_TAG + 48 <= L_BYTES, "LDS");

__device__ __forceinline__ float bf2f(u32 b) { return __uint_as_float(b << 16); }
__device__ __forceinline__ u32 f2bf(float f) { __bf16 b = (__bf16)f; return (u32)__builtin_bit_cast(u16, b); }
__device__ __forceinline__ float rbf(float f) { return bf2f(f2bf(f)); }
__device__ __forceinline__ u32 pk2(float lo, float hi) {
  bf16x2 v = {(__bf16)lo, (__bf16)hi};
  return __builtin_bit_cast(u32, v);
}
__device__ __forceinline__ float dot2(u32 a, u32 b, float acc) {
  return __builtin_amdgcn_fdot2_f32_bf16(__builtin_bit_cast(bf16x2, a), __builtin_bit_cast(bf16x2, b), acc, false);
}
template <int CTRL> __device__ __forceinline__ float dpp(float x) {
  return __builtin_bit_cast(float, __builtin_amdgcn_mov_dpp(__builtin_bit_cast(int, x), CTRL, 0xf, 0xf, true));
}
template <int CTRL, int BANK> __device__ __forceinline__ float dppm(float old, float x) {
  return __builtin_bit_cast(float, __builtin_amdgcn_update_dpp(__builtin_bit_cast(int, old), __builtin_bit_cast(int, x), CTRL, 0xf, BANK, false));
}
constexpr int XOR1 = 0xB1, XOR2 = 0x4E, ROR4 = 0x124, ROR8 = 0x128, SHL4 = 0x104, SHR4 = 0x114;
__device__ __forceinline__ f32x2 swap16(float a, float b) {
  auto s = __builtin_amdgcn_permlane16_swap(__float_as_uint(a), __float_as_uint(b), false, false);
  return f32x2{__uint_as_float(s[0]), __uint_as_float(s[1])};
}
__device__ __forceinline__ f32x2 swap32(float a, float b) {
  auto s = __builtin_amdgcn_permlane32_swap(__float_as_uint(a), __float_as_uint(b), false, false);
  return f32x2{__uint_as_float(s[0]), __uint_as_float(s[1])};
}
__device__ __forceinline__ float xrows_sum(float x) {
  f32x2 s = swap16(x, x); x = s[0] + s[1];
  f32x2 t = swap32(x, x); return t[0] + t[1];
}
__device__ __forceinline__ float wave_sum(float x) {
  x += dpp<XOR1>(x); x += dpp<XOR2>(x); x += dpp<ROR4>(x); x += dpp<ROR8>(x);
  return xrows_sum(x);
}
typedef __amdgpu_buffer_rsrc_t rsrc_t;
__device__ __forceinline__ rsrc_t mk_rsrc(const void* p, u32 bytes) { return __builtin_amdgcn_make_buffer_rsrc((void*)p, 0, (int)bytes, 0x00020000); }
#define LGKM0() asm volatile("s_waitcnt lgkmcnt(0)" ::: "memory")
__device__ __forceinline__ void wg_barrier() {
  LGKM0();
  __builtin_amdgcn_s_barrier();
  asm volatile("" ::: "memory");
}
__device__ __forceinline__ void st_gran(GAS u64* g, u32 tag, u32 v) { __hip_atomic_store(g, ((u64)tag << 32) | (u64)v, RLX_AGENT); }
__device__ __forceinline__ void lds_wait_ge(volatile LAS u32* p, u32 need) {
  for (unsigned spins = 0; (u32)__builtin_amdgcn_readfirstlane(*p) < need && spins < (SPIN_CAP << 4); ++spins) __builtin_amdgcn_s_sleep(1);
  asm volatile("" ::: "memory");
}
#define PIN8(a) asm volatile("" : "+v"(a[0]), "+v"(a[1]), "+v"(a[2]), "+v"(a[3]), "+v"(a[4]), "+v"(a[5]), "+v"(a[6]), "+v"(a[7]))
#define PIN4(a) asm volatile("" : "+v"(a[0]), "+v"(a[1]), "+v"(a[2]), "+v"(a[3]) : : "memory")

__device__ __forceinline__ float k_unit(const u32x4 (&k)[4], const u32x4& q0, const u32x4& q1) {
  float e[4];
#pragma unroll
  for (int i = 0; i < 4; ++i) {
    float a = 0.f, b = 0.f;
#pragma unroll
    for (int j = 0; j < 4; ++j) { a = dot2(k[i][j], q0[j], a); b = dot2(k[i][j], q1[j], b); }
    const f32x2 s = swap16(a, b);
    e[i] = s[0] + s[1];
  }
  const float f0 = dppm<ROR8, 0x3>(e[2], e[0]) + dppm<ROR8, 0xc>(e[0], e[2]);
  const float f1 = dppm<ROR8, 0x3>(e[3], e[1]) + dppm<ROR8, 0xc>(e[1], e[3]);
  float g = dppm<SHL4, 0x5>(f1, f0) + dppm<SHR4, 0xa>(f0, f1);
  g += dpp<XOR1>(g); g += dpp<XOR2>(g);
  return rbf(g);
}
__device__ __forceinline__ void v_piece(f32x2 (&acc)[8], float p0, float p1, u32x4 v) {
#pragma unroll
  for (int j = 0; j < 4; ++j) {
    const f32x2 vv = {__uint_as_float(v[j] << 16), __uint_as_float(v[j] & 0xffff0000u)};
    acc[j] = __builtin_elementwise_fma(vv, f32x2{p0, p0}, acc[j]);
    acc[4 + j] = __builtin_elementwise_fma(vv, f32x2{p1, p1}, acc[4 + j]);
  }
}

#define DMA_W(R, VO, SO, M0) asm volatile("s_mov_b32 m0, %0\n\ts_nop 0\n\tbuffer_load_dwordx4 %1, %2, %3 offen nt lds" :: "s"(M0), "v"(VO), "s"(R), "s"(SO) : "m0", "memory")
#define VM0() asm volatile("s_waitcnt vmcnt(0)" ::: "memory")
__device__ __forceinline__ s16x8 tr_frag(const LAS unsigned char* p, int four_rows) {
  const s16x4 b0 = __builtin_amdgcn_ds_read_tr16_b64_v4i16((LAS s16x4*)p);
  const s16x4 b1 = __builtin_amdgcn_ds_read_tr16_b64_v4i16((LAS s16x4*)(p + four_rows));
  return s16x8{b0[0], b0[1], b0[2], b0[3], b1[0], b1[1], b1[2], b1[3]};
}
__device__ __forceinline__ f32x4 mfma16(s16x8 a, s16x8 b, f32x4 c) {
  return __builtin_amdgcn_mfma_f32_16x16x32_bf16(__builtin_bit_cast(bf16x8, a), __builtin_bit_cast(bf16x8, b), c, 0, 0, 0);
}
template <int KS> __device__ __forceinline__ f32x4 gemm8x16(const LAS unsigned char* A, int astride, const LAS unsigned char* Bt, int bstride, int lane) {
  f32x4 acc = {0.f, 0.f, 0.f, 0.f};
  const int g = lane >> 4;
  const LAS unsigned char* ap = A + (lane & 7) * astride + 16 * g;
  const LAS unsigned char* bp = Bt + (8 * g + ((lane & 15) >> 2)) * bstride + 8 * (lane & 3);
#pragma unroll
  for (int ks = 0; ks < KS; ++ks) acc = mfma16(*(const LAS s16x8*)(ap + 64 * ks), tr_frag(bp + ks * 32 * bstride, 4 * bstride), acc);
  return acc;
}
__device__ __forceinline__ u32 pair_of(f32x4 acc, int lane, int& batch) {
  const int g = lane >> 4;
  const float lo = (g >> 1) ? acc[2] : acc[0], hi = (g >> 1) ? acc[3] : acc[1];
  const float send = (lane & 1) ? lo : hi, keep = (lane & 1) ? hi : lo;
  const float got = dpp<XOR1>(send);
  batch = 4 * (g & 1) + (g >> 1) * 2 + (lane & 1);
  return (lane & 1) ? pk2(got, keep) : pk2(keep, got);
}
__device__ __forceinline__ void wait_until(u64 rt0, unsigned ticks) {
  while (__builtin_amdgcn_s_memrealtime() - rt0 < ticks) __builtin_amdgcn_s_sleep(8);
}
template <int N, int SLEEP> __device__ __forceinline__ void sweep1(rsrc_t wsr, u32 voff, u32 soff, u32 stride, u32 tag, u32 (&v)[N]) {
  unsigned have = 0;
#pragma unroll
  for (int i = 0; i < N; ++i) v[i] = 0u;
  for (unsigned spins = 0;; ++spins) {
    u32x2 t[N];
    asm volatile("" ::: "memory");
#pragma unroll
    for (int i = 0; i < N; ++i) t[i] = __builtin_amdgcn_raw_buffer_load_b64(wsr, voff, ((have >> i) & 1u) ? 0x40000000u : soff + i * stride, 16);
#pragma unroll
    for (int i = 0; i < N; ++i) {
      v[i] = ((have >> i) & 1u) ? v[i] : t[i][0];
      have |= __all(t[i][1] == tag) ? 1u << i : 0u;
    }
    if (have == (1u << N) - 1u || spins > SPIN_CAP) break;
    __builtin_amdgcn_s_sleep(SLEEP);
  }
  asm volatile("" ::: "memory");
}
template <int N, int SLEEP> __device__ __forceinline__ void sweep2(rsrc_t wsr, u32 voff, u32 soff, u32 stride, u32 tag, f32x2 (&v)[N]) {
  unsigned have = 0;
#pragma unroll
  for (int i = 0; i < N; ++i) v[i] = f32x2{0.f, 0.f};
  for (unsigned spins = 0;; ++spins) {
    u32x4 t[N];
    asm volatile("" ::: "memory");
#pragma unroll
    for (int i = 0; i < N; ++i) t[i] = __builtin_amdgcn_raw_buffer_load_b128(wsr, voff, ((have >> i) & 1u) ? 0x40000000u : soff + i * stride, 16);
#pragma unroll
    for (int i = 0; i < N; ++i) {
      v[i] = ((have >> i) & 1u) ? v[i] : f32x2{__uint_as_float(t[i][0]), __uint_as_float(t[i][2])};
      have |= __all(t[i][1] == tag && t[i][3] == tag) ? 1u << i : 0u;
    }
    if (have == (1u << N) - 1u || spins > SPIN_CAP) break;
    __builtin_amdgcn_s_sleep(SLEEP);
  }
  asm volatile("" ::: "memory");
}

__device__ __forceinline__ void fast_ge(u32 addr, u32 need) {
  u32 v, cnt = SPIN_CAP << 5;
  asm volatile("1:\n\tds_read_b32 %0, %2\n\ts_sub_u32 %1, %1, 1\n\ts_cbranch_scc1 2f\n\ts_waitcnt lgkmcnt(0)\n\t"
               "v_cmp_gt_u32 vcc, %3, %0\n\ts_cbranch_vccnz 1b\n2:\n\ts_waitcnt lgkmcnt(0)"
               : "=&v"(v), "+s"(cnt) : "v"(addr), "s"(need) : "vcc", "scc", "memory");
}
__device__ __forceinline__ void fast_bit(u32 addr, u32 mask) {
  u32 v, cnt = SPIN_CAP << 5;
  asm volatile("1:\n\tds_read_b32 %0, %2\n\ts_sub_u32 %1, %1, 1\n\ts_cbranch_scc1 2f\n\ts_waitcnt lgkmcnt(0)\n\t"
               "v_and_b32 %0, %3, %0\n\tv_cmp_eq_u32 vcc, 0, %0\n\ts_cbranch_vccnz 1b\n2:\n\ts_waitcnt lgkmcnt(0)"
               : "=&v"(v), "+s"(cnt) : "v"(addr), "s"(mask) : "vcc", "scc", "memory");
}
__device__ __forceinline__ u32x4 tag_bytes(u32 tag) {
  const u32 m = tag * 0x612e7697u;
  return u32x4{m & 0xffu, (m >> 8) & 0xffu, (m >> 16) & 0xffu, m >> 24};
}
__device__ __forceinline__ u32 tagw(float v, u32 tb) { return ((__float_as_uint(v) + 0x80u) & 0xffffff00u) | tb; }
template <int N> __device__ __forceinline__ void grab(rsrc_t wsr, u32 voff, u32x4 tb, f32x4 (&v)[N]) {
  u32x4 t[N];
  for (unsigned spins = 0;; ++spins) {
    asm volatile("" ::: "memory");
#pragma unroll
    for (int i = 0; i < N; ++i) t[i] = __builtin_amdgcn_raw_buffer_load_b128(wsr, voff + i * 1024, 0, 16);
    u32 bad = 0;
#pragma unroll
    for (int i = 0; i < N; ++i) {
      t[i] ^= tb;
      bad |= (t[i][0] | t[i][1]) | (t[i][2] | t[i][3]);
    }
    if (__builtin_amdgcn_ballot_w64((bad & 0xffu) != 0u) == 0ull || spins > SPIN_CAP) break;
  }
#pragma unroll
  for (int i = 0; i < N; ++i) v[i] = __builtin_bit_cast(f32x4, t[i]);
  asm volatile("" ::: "memory");
}

__device__ __forceinline__ u32 tile_off(u32 base, u32 pair_stride, int lane) { return base + (u32)(2 * ((lane >> 4) & 1) + (lane >> 5)) * pair_stride + (lane & 15) * 8; }
__device__ __forceinline__ u32x2 tile_tag(u32x4 tb, int lane) { return (lane & 1) ? u32x2{tb[2], tb[3]} : u32x2{tb[0], tb[1]}; }
__device__ __forceinline__ void pub_tile(rsrc_t wsr, u32 voff, f32x4 a, u32x2 tt, int lane) {
  __builtin_amdgcn_raw_buffer_store_b64(u32x2{tagw(lane < 32 ? a[0] : a[2], tt[0]), tagw(lane < 32 ? a[1] : a[3], tt[1])}, wsr, voff, 0, 16);
}
__device__ __forceinline__ float tile_sum(f32x4 s) {
  const f32x2 u0 = swap32(s[0], s[2]), u1 = swap32(s[1], s[3]);
  const f32x2 w = swap16(u0[0] + u0[1], u1[0] + u1[1]);
  const float h = w[0] + w[1];
  return h + dpp<ROR8>(h);
}

__global__ void __launch_bounds__(NTHR, 3)
fused_decode(const u16* __restrict__ kc_, const u16* __restrict__ vc_, unsigned char* ws_, const u16* __restrict__ x_,
             const u16* __restrict__ wq_, const u16* __restrict__ wo_, const u16* __restrict__ w1_, const u16* __restrict__ w2_,
             u16* __restrict__ out_) {
  extern __shared__ __attribute__((aligned(16))) unsigned char smem[];
  LAS unsigned char* L = (LAS unsigned char*)smem;
  const int tid = threadIdx.x, lane = tid & 63, wave = __builtin_amdgcn_readfirstlane(tid >> 6);
  const int bid = blockIdx.x, kvh = bid & 7, jj = bid >> 3;
  const u32 ldsb = (u32)(uintptr_t)L;
  const int b = jj / 3, sp = jj - 3 * b, pair = b * 8 + kvh;
  if (jj < 24 && wave < 2) {
    const rsrc_t kr = mk_rsrc(kc_, (u32)B * HKV * SK * DH * 2);
    u32 so = (u32)pair * (SK * DH * 2) + (u32)(sp + 3 * wave) * 4096, m0 = ldsb + wave * 4096;
#pragma unroll 1
    for (int u = 0; u < WU; ++u, so += 24576, m0 += 8192)
      asm volatile("s_mov_b32 m0, %0\n\ts_nop 0\n\t"
                   "buffer_load_dwordx4 %1, %2, %3 offen nt lds\n\tbuffer_load_dwordx4 %1, %2, %3 offen offset:1024 nt lds\n\t"
                   "buffer_load_dwordx4 %1, %2, %3 offen offset:2048 nt lds\n\tbuffer_load_dwordx4 %1, %2, %3 offen offset:3072 nt lds"
                   :: "s"(m0), "v"(lane * 16), "s"(kr), "s"(so) : "m0", "memory");
  }
  u64 rt0;
  asm volatile("s_memrealtime %0\n\ts_waitcnt lgkmcnt(0)"
               : "=s"(rt0), "+s"(vc_), "+s"(ws_), "+s"(x_), "+s"(wq_), "+s"(wo_), "+s"(w1_), "+s"(w2_), "+s"(out_));
  GAS unsigned char* ws = (GAS unsigned char*)ws_;
  const rsrc_t wsr = mk_rsrc(ws_, WS_END);
  const int c = wave - 2;

  if (jj >= 24) {
    const int t = jj - 24;
    const int head = 2 * kvh + (t >> 2), cq = t & 3;
    const bool mid = wave >= 2 && wave <= 9, ffn = t < 7;
    if (mid) {
      u32x4 xw;
      asm volatile("global_load_dwordx4 %0, %1, off" : "=v"(xw) : "v"(x_ + c * D + 8 * (lane < 56 ? lane : 55)) : "memory");
      {
        const rsrc_t r = mk_rsrc(wq_, (u32)D * HQ * DH * 2);
        const u32 vo = (lane >> 3) * (HQ * DH * 2) + (lane & 7) * 16;
#pragma unroll
        for (int it = 0; it < 7; ++it) DMA_W(r, vo, (56 * c + 8 * it) * (HQ * DH * 2) + t * 1024 + kvh * 128, ldsb + S_WQ + (7 * c + it) * 1024);
      }
      asm volatile("s_waitcnt vmcnt(7)" : "+v"(xw) :: "memory");
      float xv[8];
#pragma unroll
      for (int j = 0; j < 4; ++j) { xv[2 * j] = __uint_as_float(xw[j] << 16); xv[2 * j + 1] = __uint_as_float(xw[j] & 0xffff0000u); }
      float s = ((xv[0] + xv[1]) + (xv[2] + xv[3])) + ((xv[4] + xv[5]) + (xv[6] + xv[7]));
      const float mean = wave_sum(lane < 56 ? s : 0.f) * (1.f / D);
      float ss = 0.f;
#pragma unroll
      for (int j = 0; j < 8; ++j) { const float d = xv[j] - mean; ss = fmaf(d, d, ss); }
      const float var = rbf(wave_sum(lane < 56 ? ss : 0.f) * (1.f / D));
      const float mb = rbf(mean), sd = rbf(sqrtf(rbf(var + 1e-5f)));
      u32x4 o;
#pragma unroll
      for (int j = 0; j < 4; ++j) o[j] = pk2(rbf(xv[2 * j] - mb) / sd, rbf(xv[2 * j + 1] - mb) / sd);
      if (lane < 56) *(LAS u32x4*)(L + S_XN + c * XN_STRIDE + 16 * lane) = o;
      VM0();
    } else if (wave == 10) {
      const u32 e = __hip_atomic_load((const GAS u32*)(ws + WS_EPOCH), RLX_AGENT);
      if (lane < 12) ((LAS u32*)(L + S_TAG))[lane] = lane ? 0u : e + 1u;
    }
    wg_barrier();
    const u32 tag = __builtin_amdgcn_readfirstlane(*(const LAS u32*)(L + S_TAG));
    if (wave >= 2 && wave <= 5) {
      const f32x4 acc = gemm8x16<14>(L + S_XN, XN_STRIDE, L + S_WQ + 32 * c, 128, lane);
      int bb; const u32 v = pair_of(acc, lane, bb);
      st_gran((GAS u64*)(ws + WS_H0) + (size_t)(bb * 8 + t) * 256 + 32 * kvh + 8 * c + ((lane & 15) >> 1), tag, v);
    }
    wg_barrier();
    float w0 = 0.f, w1 = 0.f, w2 = 0.f;
    const unsigned tw = T_W + c * (W_GAP / 8) + (t * W_GAP) / 64;
    if (mid) {
      const rsrc_t r1 = mk_rsrc(w1_, (u32)D * DFF * 2);
      const u32 vo1 = (lane >> 2) * (DFF * 2) + (lane & 3) * 16;
#pragma unroll 1
      for (int it = 0; it < 4; ++it) {
        wait_until(rt0, tw + it * W_GAP);
        if (ffn && c < 7) DMA_W(r1, vo1, (4 * c + it) * 16 * (DFF * 2) + kvh * 448 + t * 64, ldsb + S_W1 + (4 * c + it) * 1024);
      }
      VM0();
    }
    wg_barrier();
    if (mid) {
      const rsrc_t r2 = mk_rsrc(w2_, (u32)D * DFF * 2);
      const u32 vo2 = (lane >> 3) * (D * 2) + (lane & 7) * 16;
#pragma unroll 1
      for (int it = 0; it < 4; ++it) {
        wait_until(rt0, tw + (4 + it) * W_GAP);
        if (ffn && c < 7) DMA_W(r2, vo2, (kvh * 224 + (4 * c + it) * 8) * (D * 2) + t * 128, ldsb + S_W2 + (4 * c + it) * 1024);
      }
      VM0();
    } else if (ffn) {
      if (wave < 2) {
        f32x4 acc = gemm8x16<14>(L + S_XN, XN_STRIDE, L + S_W1 + 32 * wave, 64, lane);
#pragma unroll
        for (int i = 0; i < 4; ++i) acc[i] = fmaxf(acc[i], 0.f);
        int bb; const u32 v = pair_of(acc, lane, bb);
        st_gran((GAS u64*)(ws + WS_HH) + (size_t)(kvh * 8 + bb) * 112 + 16 * t + 8 * wave + ((lane & 15) >> 1), tag, v);
      } else {
        __builtin_amdgcn_s_sleep(127);
        f32x2 v[7];
        sweep2<7, 40>(wsr, lane * 16, WS_HH + kvh * 7168, 1024, tag, v);
#pragma unroll
        for (int i = 0; i < 7; ++i) { const int idx = lane + 64 * i, bb = idx / 56; *(LAS f32x2*)(L + S_HA + bb * HA_STRIDE + 8 * (idx - bb * 56)) = v[i]; }
      }
    }
    wg_barrier();
    const u32x4 tb = tag_bytes(tag);
    u32x2 tt = tile_tag(tb, lane);
    if (wave < 2) {
      if (ffn) {
#pragma unroll
        for (int i = 0; i < 2; ++i) {
          const f32x4 acc = gemm8x16<7>(L + S_HA, HA_STRIDE, L + S_W2 + 64 * wave + 32 * i, 128, lane);
          pub_tile(wsr, tile_off(WS_HF + ((4 * t + 2 * wave + i) * 8 + kvh) * 128, 28 * 8 * 128, lane), acc, tt, lane);
        }
      }
      return;
    }
    if (mid) {
      const rsrc_t r = mk_rsrc(wo_, (u32)D * HQ * DH * 2);
#pragma unroll 1
      for (int it = 0; it < 7; ++it) {
        const int i = lane + 64 * (7 * c + it), row = i / 14;
        wait_until(rt0, tw + (8 + it) * W_GAP);
        DMA_W(r, (u32)(row * (D * 2) + (i - row * 14) * 16), head * 256 * (D * 2) + cq * 224, ldsb + S_WQ + (7 * c + it) * 1024);
      }
      {
        u32 v[1];
        sweep1<1, 40>(wsr, (u32)((lane & 15) < 12 ? (lane & 15) : 0) * 8, WS_ML + (c * 8 + kvh) * 128, 0, tag, v);
        const int h = head & 1;
        float mv[3], lv[3];
#pragma unroll
        for (int s = 0; s < 3; ++s) {
          mv[s] = __uint_as_float(__builtin_amdgcn_readlane(v[0], 4 * s + h));
          lv[s] = __uint_as_float(__builtin_amdgcn_readlane(v[0], 4 * s + 2 + h));
        }
        const float M = fmaxf(fmaxf(mv[0], mv[1]), mv[2]);
        w0 = __builtin_amdgcn_exp2f((mv[0] - M) * CEXP); w1 = __builtin_amdgcn_exp2f((mv[1] - M) * CEXP); w2 = __builtin_amdgcn_exp2f((mv[2] - M) * CEXP);
        const float Ls = fmaf(w0, lv[0], fmaf(w1, lv[1], w2 * lv[2]));
        w0 /= Ls; w1 /= Ls; w2 /= Ls;
      }
      VM0();
    }
    wg_barrier();
    if (wave == 10) {
      GAS u64* np = (GAS u64*)(ws + WS_N2) + (((lane & 31) / 7) * 28 + 7 * cq + (lane & 31) % 7) * 16 + head;
      const u32 vo = WS_N1 + (head * 4 + cq) * 256 + ((lane & 3) < 3 ? (lane & 31) : (lane & 28)) * 8;
      for (unsigned spins = 0; spins < SPIN_CAP; ++spins) {
        asm volatile("" ::: "memory");
        const u32x2 g = __builtin_amdgcn_raw_buffer_load_b64(wsr, vo, 0, 16);
        u32 ok = (u32)__builtin_amdgcn_ballot_w64(g[1] == tag);
        ok &= ok >> 1; ok &= ok >> 2;
        *(volatile LAS u32*)(L + S_TAG + 4) = ok;
        if ((ok & 0x11111111u) == 0x11111111u) break;
      }
      *(volatile LAS u32*)(L + S_TAG + 4) = 0x11111111u;
      if (lane < 28) st_gran(np, tag, 1u);
      return;
    }
    {
      s16x8 wb[8];
      {
        const LAS unsigned char* bp = L + S_WQ + (8 * (lane >> 4) + ((lane & 15) >> 2)) * 224 + 32 * (c < 7 ? c : 6) + 8 * (lane & 3);
#pragma unroll
        for (int ks = 0; ks < 8; ++ks) wb[ks] = tr_frag(bp + ks * 32 * 224, 4 * 224);
        PIN8(wb);
      }
      u32 h1off = WS_H1 + (head * 8 + c) * 3072 + lane * 16, oaw = ldsb + S_OA + c * OA_STRIDE + 8 * lane;
      u32 oar = ldsb + S_OA + (lane & 7) * OA_STRIDE + 16 * (lane >> 4), flg = ldsb + S_TAG + 4, cntw = ldsb + S_TAG + 16 + 4 * c, cnta = ldsb + S_TAG + 16 + 4 * (lane & 7), one = 1u;
      u32 h2off = tile_off(WS_H2 + ((7 * cq + c) * 16 + head) * 128, 28 * 16 * 128, lane);
      asm volatile("" : "+v"(w0), "+v"(w1), "+v"(w2), "+v"(h1off), "+v"(oaw), "+v"(oar), "+v"(flg), "+v"(cnta), "+v"(cntw), "+v"(one), "+v"(h2off), "+v"(tt));
      fast_bit(flg, 1u << (4 * c));
      {
        f32x4 v[3];
        grab<3>(wsr, h1off, tb, v);
        const f32x4 a = __builtin_elementwise_fma(f32x4{w0, w0, w0, w0}, v[0], __builtin_elementwise_fma(f32x4{w1, w1, w1, w1}, v[1], f32x4{w2, w2, w2, w2} * v[2]));
        asm volatile("ds_write_b64 %0, %1\n\tds_write_b32 %2, %3"
                     :: "v"(oaw), "v"(u32x2{pk2(a[0], a[1]), pk2(a[2], a[3])}), "v"(cntw), "v"(one) : "memory");
      }
      if (c < 7) {
        fast_ge(cnta, 1u);
        s16x8 a[8];
#pragma unroll
        for (int ks = 0; ks < 8; ++ks) a[ks] = *(const LAS s16x8*)(uintptr_t)(oar + 64 * ks);
        __builtin_amdgcn_sched_barrier(0);
        f32x4 s0 = {0.f, 0.f, 0.f, 0.f}, s1 = {0.f, 0.f, 0.f, 0.f};
#pragma unroll
        for (int ks = 0; ks < 8; ks += 2) { s0 = mfma16(a[ks], wb[ks], s0); s1 = mfma16(a[ks + 1], wb[ks + 1], s1); }
        pub_tile(wsr, h2off, s0 + s1, tt, lane);
      }
    }
    return;
  }

  volatile LAS u32* ctl = (volatile LAS u32*)(L + L_CTL);
  const int nu = sp == 2 ? 170 : 171;

  if (wave < 2) {
    __builtin_amdgcn_s_setprio(3);
    const rsrc_t kr = mk_rsrc(kc_, (u32)B * HKV * SK * DH * 2), vr = mk_rsrc(vc_, (u32)B * HKV * SK * DH * 2);
    const u32 so0 = (u32)pair * (SK * DH * 2) + (u32)(sp + 3 * wave) * 4096, vo = lane * 16, lb = ldsb + wave * 4096;
#define DMA_UNIT(R, SO, M0, PUB) asm volatile("ds_read_b32 %0, %9\n\ts_mov_b32 m0, %2\n\t" \
      "s_waitcnt vmcnt(%6)\n\tbuffer_load_dwordx4 %3, %4, %5 offen nt lds\n\t" \
      "s_waitcnt vmcnt(%6)\n\tbuffer_load_dwordx4 %3, %4, %5 offen offset:1024 nt lds\n\t" \
      "s_waitcnt vmcnt(%6)\n\tbuffer_load_dwordx4 %3, %4, %5 offen offset:2048 nt lds\n\t" \
      "s_waitcnt vmcnt(%6)\n\ts_memrealtime %1\n\tbuffer_load_dwordx4 %3, %4, %5 offen offset:3072 nt lds\n\t" \
      "s_waitcnt lgkmcnt(0)\n\tds_write_b32 %7, %8" \
      : "=&v"(dn), "=&s"(tnow) : "s"(M0), "v"(vo), "s"(R), "s"(SO), "n"(WIN - 1), "v"(pubaddr), "v"(PUB), "v"(dnaddr) : "m0", "memory")
    u32 dn;
    u64 tnow;
    const u32 org = (u32)rt0 + PACE_A;
    const u32 dnaddr = ldsb + L_CTL + 32 + 8 * (lane & 3) + 4 * wave;
    wg_barrier();
    const u32 pubaddr = ldsb + L_CTL + 4 * wave;
#pragma unroll
    for (int u = WU; u < 8; ++u) DMA_UNIT(kr, so0 + u * 24576, lb + u * 8192, (u32)(u - WU + 1));
    u32 pub = 0, iss = 8;
    const int rem = (nu - 8 * (NGRP - 1) + 1 - wave) >> 1;
#define STEP(j) do { \
        if ((int)__builtin_amdgcn_readlane(dn, j) < need) {     \
          VM0(); pub = iss; ctl[wave] = pub; lds_wait_ge(ctl + 8 + 2 * (j) + wave, (u32)need); } \
        u32 pv = iss - WU + 1; \
        { const u32 due = org + (((ug + 6 * (j)) * (TAU * 8 / 3)) >> 8); const int lag = (int)((u32)tnow - due); \
          if (lag < LAG3) {     \
            if (lag < 0) { while ((int)((u32)__builtin_amdgcn_s_memrealtime() - due) < 0) __builtin_amdgcn_s_sleep(0); } \
            asm volatile("s_waitcnt vmcnt(%0)" :: "n"(WA + 4) : "memory"); \
            if (lag < LAG2) { asm volatile("s_waitcnt vmcnt(%0)" :: "n"(WA + 2) : "memory"); pv = iss - (WA + 5) / 4; \
              if (lag < LAG1) asm volatile("s_waitcnt vmcnt(%0)" :: "n"(WA) : "memory"); } } } \
        pub = pub > pv ? pub : pv; \
        DMA_UNIT(rr, so + (j) * 24576, m0 + (j) * 8192, pub); ++iss; } while (0)
    for (int g = 2; g < 2 * NGRP; ++g) {
      const int gk = g < NGRP ? g : g - NGRP;
      const u32 ug = (g < NGRP ? 0u : 512u) + 24u * gk + 3u * wave + sp;
      const int need = g - NSLOT / 8 + 1;
      const u32 so = so0 + (u32)gk * 98304, m0 = lb + (u32)(g & (NSLOT / 8 - 1)) * 32768;
      const rsrc_t rr = g < NGRP ? kr : vr;
      STEP(0);
      if (gk == NGRP - 1 && rem < 2) continue;
      STEP(1);
      if (gk == NGRP - 1) continue;
      STEP(2);
      STEP(3);
    }
#pragma unroll
    for (int r = WU - 1; r >= 0; --r) {
      asm volatile("s_waitcnt vmcnt(%0)" :: "n"(4 * r) : "memory");
      pub = pub > iss - r ? pub : iss - r;
      ctl[wave] = pub;
    }
    return;
  }

  if (wave == NC + 2) {
    u32 tag = __hip_atomic_load((const GAS u32*)(ws + WS_EPOCH), RLX_AGENT);
    u32 touch;
    {
      const size_t r0 = (size_t)pair * (SK * DH) + (size_t)sp * 2048, r1 = r0 + (size_t)(3 * nu - 3) * 2048;
      const u16* tp = lane == 0 ? vc_ + r0 : lane == 1 ? vc_ + r1 : kc_ + r1;
      touch = __builtin_nontemporal_load((const GAS u32*)tp);
    }
    if (lane < 16) ctl[lane] = 0u;
    wg_barrier();
    asm volatile("" : "+v"(tag));
    tag = __builtin_amdgcn_readfirstlane(tag) + 1u;
    ctl[3] = tag;
    asm volatile("" :: "v"(touch));
    wait_until(rt0, H0_AT);
    f32x2 v[2];
    sweep2<2, 16>(wsr, lane * 16, WS_H0 + pair * 2048, 1024, tag, v);
    *(LAS f32x2*)(L + L_Q + 8 * lane) = v[0];
    *(LAS f32x2*)(L + L_Q + 512 + 8 * lane) = v[1];
    LGKM0();
    ctl[2] = 1u;
    return;
  }

  const int cl = lane & 31;
  wg_barrier();
  const LAS unsigned char* rp = L + L_RING + c * 4096 + lane * 16;
  volatile LAS u32* landed = ctl + (c & 1);
  const int n0 = (c >> 1) + 1, nv = n0 + ((nu + 1 - (c & 1)) >> 1);
#define TAKE0(dst, g) do { \
    _Pragma("unroll") for (int i_ = 0; i_ < 4; ++i_) dst[i_] = *(const LAS u32x4*)(rp + ((g) & 3) * 32768 + i_ * 1024); \
    LGKM0(); ctl[8 + c] = (u32)((g) + 1); PIN4(dst); } while (0)
  LAS float* scp = (LAS float*)(L + L_SC + c * 1408);
  LAS float* scw = scp + (lane >> 5) * 8 + ((lane >> 2) & 3) * 2 + ((lane >> 4) & 1);
  const int ng = 8 * (NGRP - 1) + c < nu ? NGRP : NGRP - 1;
  float m = -INFINITY;
  scw[16 * (NGRP - 1)] = -INFINITY;
  {
    u32x4 k0[4], k1[4], k2[4], k3[4], k4[4], k5[4], k6[4], k7[4], q0, q1;
    int id0 = -1, id1 = -1, id2 = -1, id3 = -1, id4 = -1, id5 = -1, id6 = -1, id7 = -1;
    int gt = 0, np = 0;
    bool haveq = false;
#define TRY(kk, id) if (gt < ng && (int)__builtin_amdgcn_readfirstlane(*landed) >= n0 + 4 * gt) { asm volatile("" ::: "memory"); TAKE0(kk, gt); id = gt; ++gt; }
#define STAGE(kk, id) \
      if (id < 0) { TRY(kk, id) } \
      if (id >= 0 && haveq) { const float s_ = k_unit(kk, q0, q1); scw[16 * id] = s_; m = fmaxf(m, s_); asm volatile("" : "+v"(m)); \
        id = -1; ++np; if (gt > np) { TRY(kk, id) } } \
      else __builtin_amdgcn_s_sleep(1);
#pragma unroll 1
    for (unsigned spins = 0; np < ng && spins < (SPIN_CAP << 2); ++spins) {
      if (!haveq && __builtin_amdgcn_readfirstlane(ctl[2]) != 0u) {
        asm volatile("" ::: "memory");
        q0 = *(const LAS u32x4*)(L + L_Q + 16 * cl); q1 = *(const LAS u32x4*)(L + L_Q + 512 + 16 * cl);
        haveq = true;
      }
      STAGE(k0, id0) STAGE(k1, id1) STAGE(k2, id2) STAGE(k3, id3) STAGE(k4, id4) STAGE(k5, id5) STAGE(k6, id6) STAGE(k7, id7)
    }
  }
  ctl[8 + c] = (u32)NGRP;
  const u32 tag = __builtin_amdgcn_readfirstlane(ctl[3]);
  {
    m = fmaxf(m, dpp<ROR4>(m)); m = fmaxf(m, dpp<ROR8>(m));
    { const f32x2 t = swap32(m, m); m = fmaxf(t[0], t[1]); }
    const f32x2 mm = swap16(m, m);
    float l0 = 0.f, l1 = 0.f;
    LGKM0();
#pragma unroll
    for (int i = 0; i < 2; ++i) {
      if (i == 0 || lane < 24) {
        f32x4 s = *(const LAS f32x4*)(scp + 4 * (lane + 64 * i));
        s[0] = __builtin_amdgcn_exp2f((s[0] - mm[0]) * CEXP); s[1] = __builtin_amdgcn_exp2f((s[1] - mm[1]) * CEXP);
        s[2] = __builtin_amdgcn_exp2f((s[2] - mm[0]) * CEXP); s[3] = __builtin_amdgcn_exp2f((s[3] - mm[1]) * CEXP);
        *(LAS f32x4*)(scp + 4 * (lane + 64 * i)) = s;
        l0 += s[0] + s[2]; l1 += s[1] + s[3];
      }
    }
    l0 = wave_sum(l0); l1 = wave_sum(l1);
    if (lane == 0) { *(LAS f32x2*)(L + L_MX + 8 * c) = mm; *(LAS f32x2*)(L + L_MX + 64 + 8 * c) = f32x2{l0, l1}; }
    LGKM0();
    u32 old = 0;
    if (lane == 0) old = __hip_atomic_fetch_add((LAS u32*)(L + L_CTL + 16), 1u, __ATOMIC_RELAXED, __HIP_MEMORY_SCOPE_WORKGROUP);
    old = __builtin_amdgcn_readfirstlane(old);
    asm volatile("" ::: "memory");
    if (old == 7u) {
      const float mw = ((const LAS float*)(L + L_MX))[lane & 15];
      float M = mw;
      M = fmaxf(M, dpp<XOR2>(M)); M = fmaxf(M, dpp<ROR4>(M)); M = fmaxf(M, dpp<ROR8>(M));
      const float f = __builtin_amdgcn_exp2f((mw - M) * CEXP);
      float Ls = f * ((const LAS float*)(L + L_MX + 64))[lane & 15];
      Ls += dpp<XOR2>(Ls); Ls += dpp<ROR4>(Ls); Ls += dpp<ROR8>(Ls);
      if (lane < 4) st_gran((GAS u64*)(ws + WS_ML) + pair * 16 + sp * 4 + lane, tag, __float_as_uint((lane & 2) ? Ls : M));
      if (lane < 16) ((LAS float*)(L + L_MX + 128))[lane] = f;
      LGKM0();
      ctl[5] = 1u;
    }
  }
  f32x2 acc[8];
#pragma unroll
  for (int j = 0; j < 8; ++j) acc[j] = f32x2{0.f, 0.f};
  const LAS float* scr = scp + (lane >> 5) * 8;
  GAS u64* notice = (GAS u64*)(ws + WS_N1) + (kvh * 8 + (lane & 7)) * 32 + b * 4 + sp;
  const bool lastw = c == ((nu - 1) & 7);
  u32 landaddr = ldsb + L_CTL + 4 * (c & 1);
  u32 pw = ldsb + L_RING + c * 4096 + (lane >> 5) * 1024 + 32 * cl, pr = ldsb + L_RING + (c & 1) * 1024 + lane * 16;
  u32 h1off = WS_H1 + (((2 * kvh + (c & 1)) * 8 + b) * 3 + sp) * 1024 + lane * 16;
  const u32x4 tb = tag_bytes(tag);
  asm volatile("" : "+v"(landaddr), "+v"(pw), "+v"(pr), "+v"(h1off), "+v"(notice));
  float fh = 0.f;
#pragma unroll 1
  for (int g = 0; g < ng; ++g) {
    const f32x4 P0 = *(const LAS f32x4*)(scr + 16 * g), P1 = *(const LAS f32x4*)(scr + 16 * g + 4);
    if (g == NGRP - 4) { lds_wait_ge(ctl + 5, 1u); fh = ((const LAS float*)(L + L_MX + 128))[2 * c + (lane >> 5)]; }
    u32x4 v[4];
    fast_ge(landaddr, (u32)(nv + 4 * g));
    TAKE0(v, NGRP + g);
    v_piece(acc, P0[0], P0[1], v[0]); v_piece(acc, P0[2], P0[3], v[1]);
    v_piece(acc, P1[0], P1[1], v[2]); v_piece(acc, P1[2], P1[3], v[3]);
  }
  if (lastw && lane < 8) st_gran(notice, tag, 1u);
  {
    float o[8];
#pragma unroll
    for (int j = 0; j < 8; ++j) { const f32x2 t = swap32(acc[j >> 1][j & 1], acc[4 + (j >> 1)][j & 1]); o[j] = (t[0] + t[1]) * fh; }
    LAS float* dst = (LAS float*)(uintptr_t)pw;
    *(LAS f32x4*)dst = f32x4{o[0], o[1], o[2], o[3]};
    *(LAS f32x4*)(dst + 4) = f32x4{o[4], o[5], o[6], o[7]};
  }
  wg_barrier();
  if (c < 2) {
    const LAS f32x4* pp = (const LAS f32x4*)(uintptr_t)pr;
    const f32x4 o = ((pp[0] + pp[256]) + (pp[512] + pp[768])) + ((pp[1024] + pp[1280]) + (pp[1536] + pp[1792]));
    __builtin_amdgcn_raw_buffer_store_b128(u32x4{tagw(o[0], tb[0]), tagw(o[1], tb[1]), tagw(o[2], tb[2]), tagw(o[3], tb[3])}, wsr, h1off, 0, 16);
  }
  if (c == 7 && jj < 14) {
    const int f = 14 * kvh + jj, bp = f / 28, ct = f - bp * 28;
    const int oidx = (2 * bp + ((lane >> 4) & 1)) * D + 16 * ct + 2 * (lane & 7) + (lane >> 5);
    float xres = bf2f(((const GAS u16*)x_)[oidx]);
    f32x4 gv[1];
    grab<1>(wsr, WS_HF + f * 1024 + lane * 16, tb, gv);
    const float gp = rbf(tile_sum(gv[0]));
    float gate = rbf(gp / (1.f + __expf(-gp)));
    u32 noff = WS_N2 + f * 128 + (lane & 15) * 8, h2off = WS_H2 + f * 2048 + lane * 16;
    GAS u16* op = (GAS u16*)out_ + oidx;
    asm volatile("" : "+v"(noff), "+v"(h2off), "+v"(op), "+v"(gate), "+v"(xres));
    for (unsigned spins = 0; spins < SPIN_CAP; ++spins) {
      asm volatile("" ::: "memory");
      const u32x2 g = __builtin_amdgcn_raw_buffer_load_b64(wsr, noff, 0, 16);
      if (__builtin_amdgcn_ballot_w64(g[1] != tag) == 0ull) break;
    }
    __builtin_amdgcn_s_sleep(D2);
    f32x4 v[2];
    grab<2>(wsr, h2off, tb, v);
    const float r = xres + rbf(rbf(tile_sum(v[0] + v[1])) * gate);
    if (!(lane & 8)) *op = (u16)f2bf(r);
    if (f == 0 && lane == 0) __hip_atomic_store((GAS u32*)(ws + WS_EPOCH), tag, RLX_AGENT);
  }
}

extern "C" void kernel_launch(void* const* d_in, const int* in_sizes, int n_in, void* d_out, int out_size, void* d_ws,
                              size_t ws_size, hipStream_t stream) {
  (void)hipFuncSetAttribute((const void*)fused_decode, hipFuncAttributeMaxDynamicSharedMemorySize, L_BYTES);
  fused_decode<<<256, NTHR, L_BYTES, stream>>>((const u16*)d_in[1], (const u16*)d_in[2], (unsigned char*)d_ws, (const u16*)d_in[0],
                                               (const u16*)d_in[3], (const u16*)d_in[4], (const u16*)d_in[6], (const u16*)d_in[7],
                                               (u16*)d_out);
}
```
